```python
import torch

def _softmax(x, axis=-1):
    return torch.softmax(x, dim=axis)
def _gelu(x):
    return torch.nn.functional.gelu(x, approximate="tanh")
def _silu(x):
    return torch.nn.functional.silu(x)
def _sigmoid(x):
    return torch.sigmoid(x)
def _layernorm(x, axis=-1, eps=1e-5):
    m = x.mean(dim=axis, keepdim=True)
    v = x.var(dim=axis, keepdim=True, unbiased=False)
    return (x - m) / torch.sqrt(v + eps)
def _scatter_add(tgt, idx, src, axis=0):
    return tgt.clone().index_add_(axis, idx, src)
def _segment_sum(src, seg, n):
    out = torch.zeros((n,) + src.shape[1:], dtype=src.dtype)
    return out.index_add_(0, seg, src)
def _pad(x, pw):
    flat = tuple(v for pair in reversed(pw) for v in pair)
    return torch.nn.functional.pad(x, flat)

kernel_name = 'concat_heads__gqa_block_cached__sliding_block_cached__decode'

def setup_inputs():

    _w = (lambda a: (lambda t: t.to(torch.bfloat16) if t.is_floating_point() else t)(torch.as_tensor(a)))
    def _A_setup():
        B, Sq, D = 1, 1, 512
        Hq, Hkv, Sk, Dh = 32, 8, 32768, 128
        assert Hq % Hkv == 0, (Hq, Hkv)
        return {
            "x": _w((lambda *s: torch.randn(*s, dtype=torch.bfloat16) * 0.8)(B, Sq, D)),
            "k_cache": _w((lambda *s: torch.randn(*s, dtype=torch.bfloat16) * 1.6)(B, Hkv, Sk, Dh)),
            "v_cache": _w((lambda *s: torch.randn(*s, dtype=torch.bfloat16) * 0.2)(B, Hkv, Sk, Dh)),
            "wq": _w((lambda *s: torch.randn(*s, dtype=torch.bfloat16) * 1.6)(D, Hq * Dh) * (D ** -0.5)),
            "wo": _w((lambda *s: torch.randn(*s, dtype=torch.bfloat16) * 0.2)(Hq * Dh, D) * ((Hq * Dh) ** -0.5)),
        }
    _A_in = _A_setup()

    def _B_setup():
        B, Sq, D = 1, 1, 512
        Hq, Hkv, Sk, Dh = 32, 8, 32768, 128
        assert Hq % Hkv == 0, (Hq, Hkv)
        return {
            "x": _w((lambda *s: torch.randn(*s, dtype=torch.bfloat16) * 0.8)(B, Sq, D)),
            "k_cache": _w((lambda *s: torch.randn(*s, dtype=torch.bfloat16) * 1.6)(B, Hkv, Sk, Dh)),
            "v_cache": _w((lambda *s: torch.randn(*s, dtype=torch.bfloat16) * 0.2)(B, Hkv, Sk, Dh)),
            "wq": _w((lambda *s: torch.randn(*s, dtype=torch.bfloat16) * 1.6)(D, Hq * Dh) * (D ** -0.5)),
            "wo": _w((lambda *s: torch.randn(*s, dtype=torch.bfloat16) * 0.2)(Hq * Dh, D) * ((Hq * Dh) ** -0.5)),
        }
    _B_in = _B_setup()

    return {
        "A_x": _A_in["x"],
        "A_k_cache": _A_in["k_cache"],
        "A_v_cache": _A_in["v_cache"],
        "A_wq": _A_in["wq"],
        "A_wo": _A_in["wo"],
        "B_x": _B_in["x"],
        "B_k_cache": _B_in["k_cache"],
        "B_v_cache": _B_in["v_cache"],
        "B_wq": _B_in["wq"],
        "B_wo": _B_in["wo"],
    }

def reference(A_x, A_k_cache, A_v_cache, A_wq, A_wo, B_x, B_k_cache, B_v_cache, B_wq, B_wo):
    def _A_ref(x, k_cache, v_cache, wq, wo):
        B, Sq, D = x.shape
        Hq, Dh = 32, 128
        Hkv = k_cache.shape[1]
        rep = Hq // Hkv
        q = (lambda x, *d: x.permute(*d))((x @ wq).reshape(B, Sq, Hq, Dh), 0, 2, 1, 3)
        k = (lambda x, n, axis: torch.repeat_interleave(x, n, dim=axis))(k_cache, rep, axis=1)
        v = (lambda x, n, axis: torch.repeat_interleave(x, n, dim=axis))(v_cache, rep, axis=1)
        scores = torch.einsum("bhqd,bhkd->bhqk", q, k) * (Dh ** -0.5)
        p = _softmax(scores, axis=-1)
        o = torch.einsum("bhqk,bhkd->bhqd", p, v)
        return (lambda x, *d: x.permute(*d))(o, 0, 2, 1, 3).reshape(B, Sq, Hq * Dh) @ wo

    def _B_ref(x, k_cache, v_cache, wq, wo):
        B, Sq, D = x.shape
        Hq, Dh = 32, 128
        Hkv, Sk = k_cache.shape[1], k_cache.shape[2]
        W = min(4096, max(256, Sk // 4))
        rep = Hq // Hkv
        q = (lambda x, *d: x.permute(*d))((x @ wq).reshape(B, Sq, Hq, Dh), 0, 2, 1, 3)
        k = (lambda x, n, axis: torch.repeat_interleave(x, n, dim=axis))(k_cache[:, :, Sk - W:, :], rep, axis=1)
        v = (lambda x, n, axis: torch.repeat_interleave(x, n, dim=axis))(v_cache[:, :, Sk - W:, :], rep, axis=1)
        scores = torch.einsum("bhqd,bhkd->bhqk", q, k) * (Dh ** -0.5)
        p = _softmax(scores, axis=-1)
        o = torch.einsum("bhqk,bhkd->bhqd", p, v)
        return (lambda x, *d: x.permute(*d))(o, 0, 2, 1, 3).reshape(B, Sq, Hq * Dh) @ wo

    a = _A_ref(A_x, A_k_cache, A_v_cache, A_wq, A_wo)
    b = _B_ref(B_x, B_k_cache, B_v_cache, B_wq, B_wo)
    return torch.concatenate([a, b], axis=-1)
```

```cpp
#include <hip/hip_runtime.h>
#include <hip/hip_bf16.h>
#include <stdint.h>

typedef unsigned int u32;
typedef unsigned long long u64;
typedef unsigned short u16;
typedef u32 v4u __attribute__((ext_vector_type(4)));
typedef u32 v2u __attribute__((ext_vector_type(2)));
typedef float f32x4 __attribute__((ext_vector_type(4)));
typedef float f32x2 __attribute__((ext_vector_type(2)));
typedef __bf16 bf16x8 __attribute__((ext_vector_type(8)));
typedef short s16x4 __attribute__((ext_vector_type(4)));

#define KV_AUX 2

constexpr int SK = 32768, DH = 128, DM = 512, NQ = 4096;
constexpr int NWG = 256, NTHR = 512, NT = 9;
constexpr float SCALE_LOG2E = 0.088388347648318440f * 1.4426950408889634f;

constexpr int WS_EPOCH = 0;
constexpr int WS_Q = 4096;
constexpr int WS_PART = WS_Q + 8 * 2 * 4 * 512;
constexpr int PSTR = 1152;
constexpr int WS_OUT = WS_PART + 8 * 33 * 4 * PSTR;
constexpr int WS_BYTES = WS_OUT + 2 * 128 * 4096;
constexpr unsigned SPIN_CAP = 1u << 18;

struct Params {
  const u16 *xA, *kA, *vA, *wqA, *woA, *xB, *kB, *vB, *wqB, *woB;
  u16* out;
  unsigned char* ws;
};

template <int CTRL> __device__ __forceinline__ float dppf(float x) {
  return __builtin_bit_cast(float, __builtin_amdgcn_mov_dpp(__builtin_bit_cast(int, x), CTRL, 0xf, 0xf, true));
}
template <int CTRL> __device__ __forceinline__ u32 dppu(u32 x) {
  return (u32)__builtin_amdgcn_mov_dpp((int)x, CTRL, 0xf, 0xf, true);
}
constexpr int ROR4 = 0x124, ROR8 = 0x128, ROR1 = 0x121, ROR2 = 0x122;
__device__ __forceinline__ float xrow_max(float x) {
  auto s = __builtin_amdgcn_permlane16_swap(__float_as_uint(x), __float_as_uint(x), false, false);
  x = fmaxf(__uint_as_float(s[0]), __uint_as_float(s[1]));
  auto t = __builtin_amdgcn_permlane32_swap(__float_as_uint(x), __float_as_uint(x), false, false);
  return fmaxf(__uint_as_float(t[0]), __uint_as_float(t[1]));
}
__device__ __forceinline__ float xrow_sum(float x) {
  auto s = __builtin_amdgcn_permlane16_swap(__float_as_uint(x), __float_as_uint(x), false, false);
  x = __uint_as_float(s[0]) + __uint_as_float(s[1]);
  auto t = __builtin_amdgcn_permlane32_swap(__float_as_uint(x), __float_as_uint(x), false, false);
  return __uint_as_float(t[0]) + __uint_as_float(t[1]);
}
__device__ __forceinline__ float x16_sum(float x) {
  auto s = __builtin_amdgcn_permlane16_swap(__float_as_uint(x), __float_as_uint(x), false, false);
  return __uint_as_float(s[0]) + __uint_as_float(s[1]);
}
__device__ __forceinline__ float x16_max(float x) {
  auto s = __builtin_amdgcn_permlane16_swap(__float_as_uint(x), __float_as_uint(x), false, false);
  return fmaxf(__uint_as_float(s[0]), __uint_as_float(s[1]));
}
__device__ __forceinline__ float x32_sum(float x) {
  auto t = __builtin_amdgcn_permlane32_swap(__float_as_uint(x), __float_as_uint(x), false, false);
  return __uint_as_float(t[0]) + __uint_as_float(t[1]);
}
__device__ __forceinline__ float bflo(u32 w) { return __builtin_bit_cast(float, w << 16); }
__device__ __forceinline__ float bfhi(u32 w) { return __builtin_bit_cast(float, w & 0xffff0000u); }
__device__ __forceinline__ u32 cvtpk(float lo, float hi) {
  u32 r; asm("v_cvt_pk_bf16_f32 %0, %1, %2" : "=v"(r) : "v"(lo), "v"(hi)); return r;
}
__device__ __forceinline__ u32 vmov(u32 x) { u32 r; asm volatile("v_mov_b32 %0, %1" : "=&v"(r) : "v"(x)); return r; }
#define FENCE_SCHED() do { asm volatile("" ::: "memory"); __builtin_amdgcn_sched_barrier(0); } while (0)

__global__ void __launch_bounds__(NTHR, 2) fused_decode_kernel(Params P) {
  __shared__ __attribute__((aligned(16))) float lds_red[32 * 32];
  __shared__ __attribute__((aligned(16))) float lds_mx[2 * 4 * 8];
  __shared__ __attribute__((aligned(16))) float lds_l[2 * 4 * 8];
  __shared__ __attribute__((aligned(16))) float lds_o[2 * 8 * 512];
  __shared__ __attribute__((aligned(16))) float lds_cv[29 * 34];
  __shared__ __attribute__((aligned(16))) float lds_cml[32 * 2];
  __shared__ __attribute__((aligned(16))) float lds_fin[8 * 16];

  const int tid = threadIdx.x, lane = tid & 63;
  const int w = __builtin_amdgcn_readfirstlane(tid >> 6);
  const int g = blockIdx.x & 7, i = blockIdx.x >> 3;
  const __amdgpu_buffer_rsrc_t ws = __builtin_amdgcn_make_buffer_rsrc((void*)P.ws, 0, WS_BYTES, 0x00020000);

  const v2u ep = __builtin_amdgcn_raw_buffer_load_b64(ws, WS_EPOCH, 0, 16);

  const int pq = i >> 4, hq = (i >> 2) & 3, dq = i & 3;
  const u16* xq = pq ? P.xB : P.xA;
  const u16* wqp = (pq ? P.wqB : P.wqA) + (4 * g + hq) * DH + 32 * dq;
  const int row0 = 64 * w + 4 * (lane >> 2);
  const v2u xv = *reinterpret_cast<const v2u*>(xq + row0);
  v4u wv[4];
#pragma unroll
  for (int k = 0; k < 4; ++k) wv[k] = *reinterpret_cast<const v4u*>(wqp + (size_t)(row0 + k) * NQ + 8 * (lane & 3));
  FENCE_SCHED();

  const int p1 = (i >= 29) ? 1 : 0, p2 = (i >= 28) ? 1 : 0;
  const u32 key1 = (u32)g * SK + 128 * (9 * i) + 16 * w - (p1 ? 4096 : 0);
  const u32 key2 = (u32)g * SK + 128 * (9 * i) + 16 * w - (p2 ? 4096 : 0);
  constexpr u32 KVBYTES = 8u * SK * DH * 2u;
  const __amdgpu_buffer_rsrc_t rk1 = __builtin_amdgcn_make_buffer_rsrc((void*)((p1 ? P.kB : P.kA) + (size_t)key1 * DH), 0, KVBYTES - key1 * 256u, 0x00020000);
  const __amdgpu_buffer_rsrc_t rk2 = __builtin_amdgcn_make_buffer_rsrc((void*)((p2 ? P.kB : P.kA) + (size_t)key2 * DH), 0, KVBYTES - key2 * 256u, 0x00020000);
  const __amdgpu_buffer_rsrc_t rv1 = __builtin_amdgcn_make_buffer_rsrc((void*)((p1 ? P.vB : P.vA) + (size_t)key1 * DH), 0, KVBYTES - key1 * 256u, 0x00020000);
  const __amdgpu_buffer_rsrc_t rv2 = __builtin_amdgcn_make_buffer_rsrc((void*)((p2 ? P.vB : P.vA) + (size_t)key2 * DH), 0, KVBYTES - key2 * 256u, 0x00020000);
  const int koff = ((lane >> 2) & 3) * 256 + ((lane >> 4) * 4 + (lane & 3)) * 16;
  const int voff = lane * 16;
  v4u K[NT][4], V[NT][4];
#define ISSUE_K(k_) do { _Pragma("unroll") for (int t_ = 0; t_ < 4; ++t_) \
    K[k_][t_] = __builtin_amdgcn_raw_buffer_load_b128((k_) < 4 ? rk1 : rk2, koff + t_ * 1024, (k_) * 32768, KV_AUX); FENCE_SCHED(); } while (0)
#define ISSUE_V(k_) do { _Pragma("unroll") for (int t_ = 0; t_ < 4; ++t_) \
    V[k_][t_] = __builtin_amdgcn_raw_buffer_load_b128((k_) < 4 ? rv1 : rv2, voff + t_ * 1024, (k_) * 32768, KV_AUX); FENCE_SCHED(); } while (0)
  ISSUE_K(0); ISSUE_K(1);

  {
    float acc[8];
#pragma unroll
    for (int j = 0; j < 8; ++j) acc[j] = 0.f;
    const float xf[4] = {bflo(xv.x), bfhi(xv.x), bflo(xv.y), bfhi(xv.y)};
#pragma unroll
    for (int k = 0; k < 4; ++k)
#pragma unroll
      for (int j = 0; j < 4; ++j) {
        acc[2 * j] = fmaf(xf[k], bflo(wv[k][j]), acc[2 * j]);
        acc[2 * j + 1] = fmaf(xf[k], bfhi(wv[k][j]), acc[2 * j + 1]);
      }
#pragma unroll
    for (int j = 0; j < 8; ++j) { acc[j] += dppf<ROR4>(acc[j]); acc[j] += dppf<ROR8>(acc[j]); }
    if (((lane >> 2) & 3) == 0) {
      float* dst = lds_red + (w * 4 + (lane >> 4)) * 32 + 8 * (lane & 3);
      *reinterpret_cast<f32x4*>(dst) = f32x4{acc[0], acc[1], acc[2], acc[3]};
      *reinterpret_cast<f32x4*>(dst + 4) = f32x4{acc[4], acc[5], acc[6], acc[7]};
    }
  }
  __syncthreads();
  const u32 tag0 = __builtin_amdgcn_readfirstlane(ep.x) + 1u, tag1 = __builtin_amdgcn_readfirstlane(ep.y) + 1u;
  if (w == 0) {
    const int col = lane & 31, half = lane >> 5;
    float s = 0.f;
#pragma unroll
    for (int r = 0; r < 16; ++r) s += lds_red[(16 * half + r) * 32 + col];
    s = x32_sum(s);
    const u32 b = cvtpk(s, s) & 0xffffu;
    const u32 pair = b | (dppu<0xF5>(b) << 16);
    const u32 pair2 = dppu<0xAA>(pair);
    if ((lane & 3) == 0 && lane < 32) {
      const v4u gr = {pair, tag0, pair2, tag1};
      __builtin_amdgcn_raw_buffer_store_b128(gr, ws, WS_Q + ((g * 2 + pq) * 4 + hq) * 512 + (8 * dq + (lane >> 2)) * 16, 0, 16);
    }
  }
  FENCE_SCHED();

  const int qh = (lane >> 2) & 3, qch = 4 * (lane >> 4) + (lane & 3);
  const int qoff1 = WS_Q + ((g * 2 + p1) * 4 + qh) * 512 + qch * 32;
  const int qoff2 = WS_Q + ((g * 2 + p2) * 4 + qh) * 512 + qch * 32;
  v4u qa0, qa1, qb0, qb1;
#define POLLQ() do { qa0 = __builtin_amdgcn_raw_buffer_load_b128(ws, qoff1, 0, 16); qa1 = __builtin_amdgcn_raw_buffer_load_b128(ws, qoff1 + 16, 0, 16); \
    qb0 = __builtin_amdgcn_raw_buffer_load_b128(ws, qoff2, 0, 16); qb1 = __builtin_amdgcn_raw_buffer_load_b128(ws, qoff2 + 16, 0, 16); FENCE_SCHED(); } while (0)
#define CHECKQ() do { ok = (__all(qa0.y == tag0 && qa0.w == tag1 && qa1.y == tag0 && qa1.w == tag1 && qb0.y == tag0 && qb0.w == tag1 && qb1.y == tag0 && qb1.w == tag1) != 0); \
    q1 = v4u{vmov(qa0.x), vmov(qa0.z), vmov(qa1.x), vmov(qa1.z)}; q2 = v4u{vmov(qb0.x), vmov(qb0.z), vmov(qb1.x), vmov(qb1.z)}; FENCE_SCHED(); } while (0)
  bool ok;
  v4u q1, q2;
  POLLQ(); ISSUE_K(2); ISSUE_K(3); CHECKQ();
  if (!ok) POLLQ();
  ISSUE_K(4); ISSUE_K(5);
  if (!ok) CHECKQ();
  if (!ok) POLLQ();
  ISSUE_K(6); ISSUE_K(7);
  if (!ok) CHECKQ();
  if (!ok) POLLQ();
  ISSUE_K(8);
  if (!ok) CHECKQ();
  for (unsigned spins = 0; !ok && spins < SPIN_CAP; ++spins) { __builtin_amdgcn_s_sleep(1); POLLQ(); CHECKQ(); }

  float s[NT][4];
#pragma unroll
  for (int k = 0; k < NT; ++k) {
#pragma unroll
    for (int t = 0; t < 4; ++t) {
      const f32x4 d = __builtin_amdgcn_mfma_f32_16x16x32_bf16(__builtin_bit_cast(bf16x8, K[k][t]), __builtin_bit_cast(bf16x8, k < 4 ? q1 : q2),
                                                               f32x4{0.f, 0.f, 0.f, 0.f}, 0, 0, 0);
      s[k][t] = (dppf<0x00>(d[0]) + dppf<0x55>(d[1])) + (dppf<0xAA>(d[2]) + dppf<0xFF>(d[3]));
    }
    FENCE_SCHED();
    ISSUE_V(k);
  }
  const int po = i >> 4, ho = (i >> 2) & 3, qo = i & 3;
  const u16* wop = (po ? P.woB : P.woA) + (size_t)((4 * g + ho) * DH + 32 * qo + 4 * w) * DM + 8 * lane;
  v4u wo[4];
#pragma unroll
  for (int k = 0; k < 4; ++k) wo[k] = *reinterpret_cast<const v4u*>(wop + k * DM);
  FENCE_SCHED();

  {
    float m1 = s[0][0], m2 = s[4][0];
#pragma unroll
    for (int k = 0; k < 4; ++k)
#pragma unroll
      for (int t = 0; t < 4; ++t) m1 = fmaxf(m1, s[k][t]);
#pragma unroll
    for (int k = 4; k < NT; ++k)
#pragma unroll
      for (int t = 0; t < 4; ++t) m2 = fmaxf(m2, s[k][t]);
    m1 = xrow_max(m1); m2 = xrow_max(m2);
    if ((lane & 0x33) == 0) { lds_mx[(0 * 4 + qh) * 8 + w] = m1; lds_mx[(1 * 4 + qh) * 8 + w] = m2; }
  }
  __syncthreads();
  float M1, M2;
  {
    const f32x4 a = *reinterpret_cast<const f32x4*>(lds_mx + qh * 8), b = *reinterpret_cast<const f32x4*>(lds_mx + qh * 8 + 4);
    const f32x4 c = *reinterpret_cast<const f32x4*>(lds_mx + (4 + qh) * 8), d = *reinterpret_cast<const f32x4*>(lds_mx + (4 + qh) * 8 + 4);
    M1 = fmaxf(fmaxf(fmaxf(a[0], a[1]), fmaxf(a[2], a[3])), fmaxf(fmaxf(b[0], b[1]), fmaxf(b[2], b[3])));
    M2 = fmaxf(fmaxf(fmaxf(c[0], c[1]), fmaxf(c[2], c[3])), fmaxf(fmaxf(d[0], d[1]), fmaxf(d[2], d[3])));
    if (p1 == p2) { M1 = fmaxf(M1, M2); M2 = M1; }
  }
  const float Mc1 = M1 * SCALE_LOG2E, Mc2 = M2 * SCALE_LOG2E;

  const u32 hm = (lane & 1) ? 0xffff0000u : 0x0000ffffu;
  const u32 mask0 = (lane & 2) ? 0u : hm, mask1 = (lane & 2) ? hm : 0u;
  float l1 = 0.f, l2 = 0.f;
  f32x4 O[2][2];
#pragma unroll
  for (int a = 0; a < 2; ++a)
#pragma unroll
    for (int b = 0; b < 2; ++b) O[a][b] = f32x4{0.f, 0.f, 0.f, 0.f};
  v2u A[NT][4];
#define PREP(k_) do { _Pragma("unroll") for (int t_ = 0; t_ < 4; ++t_) { \
      const float p_ = __builtin_amdgcn_exp2f(fmaf(s[k_][t_], SCALE_LOG2E, (k_) < 4 ? -Mc1 : -Mc2)); \
      if ((k_) < 4) l1 += p_; else l2 += p_; \
      const u32 pk_ = cvtpk(p_, p_); A[k_][t_] = v2u{pk_ & mask0, pk_ & mask1}; } } while (0)
  PREP(0);
#pragma unroll
  for (int k = 0; k < NT; ++k) {
    if (k + 1 < NT) PREP(k + 1);
    FENCE_SCHED();
#pragma unroll
    for (int t = 0; t < 4; ++t) {
      const s16x4 a = __builtin_bit_cast(s16x4, A[k][t]);
      const int sg = k < 4 ? 0 : 1;
      O[sg][0] = __builtin_amdgcn_mfma_f32_16x16x16bf16_1k(a, __builtin_bit_cast(s16x4, v2u{V[k][t].x, V[k][t].y}), O[sg][0], 0, 0, 0);
      O[sg][1] = __builtin_amdgcn_mfma_f32_16x16x16bf16_1k(a, __builtin_bit_cast(s16x4, v2u{V[k][t].z, V[k][t].w}), O[sg][1], 0, 0, 0);
    }
    FENCE_SCHED();
  }
  const bool mixed = (p1 != p2);
  if (!mixed) { O[0][0] += O[1][0]; O[0][1] += O[1][1]; l1 += l2; }
  l1 = xrow_sum(l1); l2 = xrow_sum(l2);
  if ((lane & 0x33) == 0) { lds_l[(0 * 4 + qh) * 8 + w] = l1; lds_l[(1 * 4 + qh) * 8 + w] = l2; }
  {
    float* dst = lds_o + w * 512 + (lane >> 4) * 128 + (lane & 15) * 8;
    *reinterpret_cast<f32x4*>(dst) = O[0][0];
    *reinterpret_cast<f32x4*>(dst + 4) = O[0][1];
    if (mixed) {
      *reinterpret_cast<f32x4*>(dst + 4096) = O[1][0];
      *reinterpret_cast<f32x4*>(dst + 4096 + 4) = O[1][1];
    }
  }
  __syncthreads();
  {
    const int sg = tid >> 8;
    if (sg == 0 || mixed) {
      const int h = (tid >> 6) & 3, gm = tid & 63;
      const int pout = sg ? p2 : p1;
      const int slot = i + pout;
      f32x2 a = {0.f, 0.f};
#pragma unroll
      for (int ww = 0; ww < 8; ++ww) a += *reinterpret_cast<const f32x2*>(lds_o + sg * 4096 + ww * 512 + h * 128 + 2 * gm);
      const int base = WS_PART + ((g * 33 + slot) * 4 + h) * PSTR;
      const v4u gr = {__float_as_uint(a[0]), tag0, __float_as_uint(a[1]), tag1};
      __builtin_amdgcn_raw_buffer_store_b128(gr, ws, base + gm * 16, 0, 16);
      if (gm == 0) {
        const f32x4 la = *reinterpret_cast<const f32x4*>(lds_l + (sg * 4 + h) * 8), lb = *reinterpret_cast<const f32x4*>(lds_l + (sg * 4 + h) * 8 + 4);
        const float L = ((la[0] + la[1]) + (la[2] + la[3])) + ((lb[0] + lb[1]) + (lb[2] + lb[3]));
        const f32x4 a0 = *reinterpret_cast<const f32x4*>(lds_mx + h * 8), b0 = *reinterpret_cast<const f32x4*>(lds_mx + h * 8 + 4);
        const f32x4 c0 = *reinterpret_cast<const f32x4*>(lds_mx + (4 + h) * 8), d0 = *reinterpret_cast<const f32x4*>(lds_mx + (4 + h) * 8 + 4);
        float Ma = fmaxf(fmaxf(fmaxf(a0[0], a0[1]), fmaxf(a0[2], a0[3])), fmaxf(fmaxf(b0[0], b0[1]), fmaxf(b0[2], b0[3])));
        float Mb = fmaxf(fmaxf(fmaxf(c0[0], c0[1]), fmaxf(c0[2], c0[3])), fmaxf(fmaxf(d0[0], d0[1]), fmaxf(d0[2], d0[3])));
        const float Mx = mixed ? (sg ? Mb : Ma) : fmaxf(Ma, Mb);
        const v4u gm_ = {__float_as_uint(Mx * SCALE_LOG2E), tag0, __float_as_uint(L), tag1};
        __builtin_amdgcn_raw_buffer_store_b128(gm_, ws, base + 64 * 16, 0, 16);
      }
    }
  }
  FENCE_SCHED();

  const int nprod = po ? 4 : 29, slot0 = po ? 29 : 0;
  {
    const bool isdata = tid < 16 * nprod, isml = !isdata && tid < 17 * nprod;
    const int j = isdata ? (tid >> 4) : (tid - 16 * nprod);
    const int off = WS_PART + ((g * 33 + slot0 + j) * 4 + ho) * PSTR + (isdata ? (16 * qo + (tid & 15)) * 16 : 64 * 16);
    if (64 * w < 17 * nprod) {
      v4u v;
      for (unsigned spins = 0; spins < SPIN_CAP; ++spins) {
        v = __builtin_amdgcn_raw_buffer_load_b128(ws, (isdata || isml) ? off : WS_EPOCH + 64, 0, 16);
        if (__all(!(isdata || isml) || (v.y == tag0 && v.w == tag1))) break;
        __builtin_amdgcn_s_sleep(1);
      }
      if (isdata) *reinterpret_cast<f32x2*>(lds_cv + j * 34 + 2 * (tid & 15)) = f32x2{__uint_as_float(v.x), __uint_as_float(v.z)};
      else if (isml) *reinterpret_cast<f32x2*>(lds_cml + 2 * j) = f32x2{__uint_as_float(v.x), __uint_as_float(v.z)};
    }
  }
  __syncthreads();
  {
    const int j = lane & 31, half = lane >> 5;
    const bool val = j < nprod;
    const int jj = val ? j : 0;
    const f32x2 ml = *reinterpret_cast<const f32x2*>(lds_cml + 2 * jj);
    const f32x2 vv = *reinterpret_cast<const f32x2*>(lds_cv + jj * 34 + 4 * w + 2 * half);
    float m = val ? ml[0] : -INFINITY;
    float M = m;
    M = fmaxf(M, dppf<ROR1>(M)); M = fmaxf(M, dppf<ROR2>(M)); M = fmaxf(M, dppf<ROR4>(M)); M = fmaxf(M, dppf<ROR8>(M)); M = x16_max(M);
    const float wt = val ? __builtin_amdgcn_exp2f(m - M) : 0.f;
    float L = wt * ml[1], oa = wt * vv[0], ob = wt * vv[1];
    L += dppf<ROR1>(L); oa += dppf<ROR1>(oa); ob += dppf<ROR1>(ob);
    L += dppf<ROR2>(L); oa += dppf<ROR2>(oa); ob += dppf<ROR2>(ob);
    L += dppf<ROR4>(L); oa += dppf<ROR4>(oa); ob += dppf<ROR4>(ob);
    L += dppf<ROR8>(L); oa += dppf<ROR8>(oa); ob += dppf<ROR8>(ob);
    L = x16_sum(L); oa = x16_sum(oa); ob = x16_sum(ob);
    const float inv = 1.f / L;
    oa *= inv; ob *= inv;
    float o4[4];
    o4[0] = __builtin_bit_cast(float, __builtin_amdgcn_readlane(__builtin_bit_cast(int, oa), 0));
    o4[1] = __builtin_bit_cast(float, __builtin_amdgcn_readlane(__builtin_bit_cast(int, ob), 0));
    o4[2] = __builtin_bit_cast(float, __builtin_amdgcn_readlane(__builtin_bit_cast(int, oa), 32));
    o4[3] = __builtin_bit_cast(float, __builtin_amdgcn_readlane(__builtin_bit_cast(int, ob), 32));
    float acc[8];
#pragma unroll
    for (int c = 0; c < 8; ++c) acc[c] = 0.f;
#pragma unroll
    for (int k = 0; k < 4; ++k)
#pragma unroll
      for (int c = 0; c < 4; ++c) {
        acc[2 * c] = fmaf(o4[k], bflo(wo[k][c]), acc[2 * c]);
        acc[2 * c + 1] = fmaf(o4[k], bfhi(wo[k][c]), acc[2 * c + 1]);
      }
    float* dst = lds_o + w * 512 + lane * 8;
    *reinterpret_cast<f32x4*>(dst) = f32x4{acc[0], acc[1], acc[2], acc[3]};
    *reinterpret_cast<f32x4*>(dst + 4) = f32x4{acc[4], acc[5], acc[6], acc[7]};
  }
  __syncthreads();
  if (tid < 256) {
    f32x2 a = {0.f, 0.f};
#pragma unroll
    for (int ww = 0; ww < 8; ++ww) a += *reinterpret_cast<const f32x2*>(lds_o + ww * 512 + 2 * tid);
    const v4u gr = {__float_as_uint(a[0]), tag0, __float_as_uint(a[1]), tag1};
    __builtin_amdgcn_raw_buffer_store_b128(gr, ws, WS_OUT + ((po * 128 + g * 16 + (i & 15)) * 256 + tid) * 16, 0, 16);
  }
  FENCE_SCHED();

  if (blockIdx.x < 64) {
    const int f = blockIdx.x;
    const int pr = tid >> 2, gm = tid & 3;
    const int offA = WS_OUT + ((0 * 128 + pr) * 256 + 4 * f + gm) * 16, offB = WS_OUT + ((1 * 128 + pr) * 256 + 4 * f + gm) * 16;
    v4u va, vb;
    for (unsigned spins = 0; spins < SPIN_CAP; ++spins) {
      va = __builtin_amdgcn_raw_buffer_load_b128(ws, offA, 0, 16);
      vb = __builtin_amdgcn_raw_buffer_load_b128(ws, offB, 0, 16);
      if (__all(va.y == tag0 && va.w == tag1 && vb.y == tag0 && vb.w == tag1)) break;
      __builtin_amdgcn_s_sleep(1);
    }
    float r[4] = {__uint_as_float(va.x), __uint_as_float(va.z), __uint_as_float(vb.x), __uint_as_float(vb.z)};
#pragma unroll
    for (int c = 0; c < 4; ++c) { r[c] += dppf<ROR4>(r[c]); r[c] += dppf<ROR8>(r[c]); r[c] = xrow_sum(r[c]); }
    if (lane < 4) {
      *reinterpret_cast<f32x2*>(lds_fin + w * 16 + 0 * 8 + gm * 2) = f32x2{r[0], r[1]};
      *reinterpret_cast<f32x2*>(lds_fin + w * 16 + 1 * 8 + gm * 2) = f32x2{r[2], r[3]};
    }
    __syncthreads();
    if (tid < 16) {
      float a = 0.f;
#pragma unroll
      for (int ww = 0; ww < 8; ++ww) a += lds_fin[ww * 16 + tid];
      const int p = tid >> 3, c = tid & 7;
      P.out[p * 512 + 8 * f + c] = (u16)(cvtpk(a, a) & 0xffffu);
    }
    if (f == 0 && tid == 0) {
      const v2u ne = {tag0, tag1};
      __builtin_amdgcn_raw_buffer_store_b64(ne, ws, WS_EPOCH, 0, 16);
    }
  }
}

extern "C" void kernel_launch(void* const* d_in, const int* in_sizes, int n_in, void* d_out, int out_size, void* d_ws, size_t ws_size,
                              hipStream_t stream) {
  Params P;
  P.xA = (const u16*)d_in[0]; P.kA = (const u16*)d_in[1]; P.vA = (const u16*)d_in[2]; P.wqA = (const u16*)d_in[3]; P.woA = (const u16*)d_in[4];
  P.xB = (const u16*)d_in[5]; P.kB = (const u16*)d_in[6]; P.vB = (const u16*)d_in[7]; P.wqB = (const u16*)d_in[8]; P.woB = (const u16*)d_in[9];
  P.out = (u16*)d_out; P.ws = (unsigned char*)d_ws;
  hipLaunchKernelGGL(fused_decode_kernel, dim3(NWG), dim3(NTHR), 0, stream, P);
}
```

```cpp
#include <hip/hip_runtime.h>
#include <hip/hip_bf16.h>
#include <stdint.h>

typedef unsigned int u32;
typedef unsigned long long u64;
typedef unsigned short u16;
typedef u32 v4u __attribute__((ext_vector_type(4)));
typedef u32 v2u __attribute__((ext_vector_type(2)));
typedef float f32x4 __attribute__((ext_vector_type(4)));
typedef float f32x2 __attribute__((ext_vector_type(2)));
typedef __bf16 bf16x8 __attribute__((ext_vector_type(8)));
typedef short s16x4 __attribute__((ext_vector_type(4)));

#define KV_AUX 2

constexpr int SK = 32768, DH = 128, DM = 512, NQ = 4096;
constexpr int NWG = 256, NTHR = 512, NT = 9;
constexpr float SCALE = 0.088388347648318440f;
constexpr float LOG2E = 1.4426950408889634f;
#define ROUND_S 1

constexpr int WS_EPOCH = 0;
constexpr int WS_Q = 4096;
constexpr int WS_PART = WS_Q + 8 * 2 * 4 * 512;
constexpr int PSTR = 1152;
constexpr int WS_OUT = WS_PART + 8 * 33 * 4 * PSTR;
constexpr int WS_BYTES = WS_OUT + 2 * 128 * 4096;
constexpr unsigned SPIN_CAP = 1u << 18;

struct Params {
  const u16 *xA, *kA, *vA, *wqA, *woA, *xB, *kB, *vB, *wqB, *woB;
  u16* out;
  unsigned char* ws;
};

template <int CTRL> __device__ __forceinline__ float dppf(float x) {
  return __builtin_bit_cast(float, __builtin_amdgcn_mov_dpp(__builtin_bit_cast(int, x), CTRL, 0xf, 0xf, true));
}
template <int CTRL> __device__ __forceinline__ u32 dppu(u32 x) {
  return (u32)__builtin_amdgcn_mov_dpp((int)x, CTRL, 0xf, 0xf, true);
}
constexpr int ROR4 = 0x124, ROR8 = 0x128, ROR1 = 0x121, ROR2 = 0x122;
__device__ __forceinline__ float xrow_max(float x) {
  auto s = __builtin_amdgcn_permlane16_swap(__float_as_uint(x), __float_as_uint(x), false, false);
  x = fmaxf(__uint_as_float(s[0]), __uint_as_float(s[1]));
  auto t = __builtin_amdgcn_permlane32_swap(__float_as_uint(x), __float_as_uint(x), false, false);
  return fmaxf(__uint_as_float(t[0]), __uint_as_float(t[1]));
}
__device__ __forceinline__ float xrow_sum(float x) {
  auto s = __builtin_amdgcn_permlane16_swap(__float_as_uint(x), __float_as_uint(x), false, false);
  x = __uint_as_float(s[0]) + __uint_as_float(s[1]);
  auto t = __builtin_amdgcn_permlane32_swap(__float_as_uint(x), __float_as_uint(x), false, false);
  return __uint_as_float(t[0]) + __uint_as_float(t[1]);
}
__device__ __forceinline__ float x16_sum(float x) {
  auto s = __builtin_amdgcn_permlane16_swap(__float_as_uint(x), __float_as_uint(x), false, false);
  return __uint_as_float(s[0]) + __uint_as_float(s[1]);
}
__device__ __forceinline__ float x16_max(float x) {
  auto s = __builtin_amdgcn_permlane16_swap(__float_as_uint(x), __float_as_uint(x), false, false);
  return fmaxf(__uint_as_float(s[0]), __uint_as_float(s[1]));
}
__device__ __forceinline__ float x32_sum(float x) {
  auto t = __builtin_amdgcn_permlane32_swap(__float_as_uint(x), __float_as_uint(x), false, false);
  return __uint_as_float(t[0]) + __uint_as_float(t[1]);
}
__device__ __forceinline__ float bflo(u32 w) { return __builtin_bit_cast(float, w << 16); }
__device__ __forceinline__ float bfhi(u32 w) { return __builtin_bit_cast(float, w & 0xffff0000u); }
__device__ __forceinline__ u32 cvtpk(float lo, float hi) {
  u32 r; asm("v_cvt_pk_bf16_f32 %0, %1, %2" : "=v"(r) : "v"(lo), "v"(hi)); return r;
}
__device__ __forceinline__ u32 vmov(u32 x) { u32 r; asm volatile("v_mov_b32 %0, %1" : "=&v"(r) : "v"(x)); return r; }
#define STAMP(n_) do { } while (0)
#define STAMP_DECL
#define STAMP_FLUSH() do { } while (0)
#define FENCE_SCHED() do { asm volatile("" ::: "memory"); __builtin_amdgcn_sched_barrier(0); } while (0)

__global__ void __launch_bounds__(NTHR, 2) fused_decode_kernel(Params P) {
  __shared__ __attribute__((aligned(16))) float lds_red[32 * 32];
  __shared__ __attribute__((aligned(16))) float lds_mx[2 * 4 * 8];
  __shared__ __attribute__((aligned(16))) float lds_l[2 * 4 * 8];
  __shared__ __attribute__((aligned(16))) float lds_o[2 * 8 * 512];
  __shared__ __attribute__((aligned(16))) float lds_cv[29 * 34];
  __shared__ __attribute__((aligned(16))) float lds_cml[32 * 2];
  __shared__ __attribute__((aligned(16))) float lds_fin[8 * 16];
  __shared__ __attribute__((aligned(16))) u32 lds_q[512];

  const int tid = threadIdx.x, lane = tid & 63;
  const int w = __builtin_amdgcn_readfirstlane(tid >> 6);
  const int g = blockIdx.x & 7, i = blockIdx.x >> 3;
  const __amdgpu_buffer_rsrc_t ws = __builtin_amdgcn_make_buffer_rsrc((void*)P.ws, 0, WS_BYTES, 0x00020000);

  STAMP_DECL;
  STAMP(0);
  const v2u ep = __builtin_amdgcn_raw_buffer_load_b64(ws, WS_EPOCH, 0, 16);

  const int pq = i >> 4, hq = (i >> 2) & 3, dq = i & 3;
  const u16* xq = pq ? P.xB : P.xA;
  const u16* wqp = (pq ? P.wqB : P.wqA) + (4 * g + hq) * DH + 32 * dq;
  const int row0 = 64 * w + 4 * (lane >> 2);
  const v2u xv = *reinterpret_cast<const v2u*>(xq + row0);
  v4u wv[4];
#pragma unroll
  for (int k = 0; k < 4; ++k) wv[k] = *reinterpret_cast<const v4u*>(wqp + (size_t)(row0 + k) * NQ + 8 * (lane & 3));
  FENCE_SCHED();

  const int p1 = (i >= 29) ? 1 : 0, p2 = (i >= 28) ? 1 : 0;
  const u32 key1 = (u32)g * SK + 128 * (9 * i) + 16 * w - (p1 ? 4096 : 0);
  const u32 key2 = (u32)g * SK + 128 * (9 * i) + 16 * w - (p2 ? 4096 : 0);
  constexpr u32 KVBYTES = 8u * SK * DH * 2u;
  const __amdgpu_buffer_rsrc_t rk1 = __builtin_amdgcn_make_buffer_rsrc((void*)((p1 ? P.kB : P.kA) + (size_t)key1 * DH), 0, KVBYTES - key1 * 256u, 0x00020000);
  const __amdgpu_buffer_rsrc_t rk2 = __builtin_amdgcn_make_buffer_rsrc((void*)((p2 ? P.kB : P.kA) + (size_t)key2 * DH), 0, KVBYTES - key2 * 256u, 0x00020000);
  const __amdgpu_buffer_rsrc_t rv1 = __builtin_amdgcn_make_buffer_rsrc((void*)((p1 ? P.vB : P.vA) + (size_t)key1 * DH), 0, KVBYTES - key1 * 256u, 0x00020000);
  const __amdgpu_buffer_rsrc_t rv2 = __builtin_amdgcn_make_buffer_rsrc((void*)((p2 ? P.vB : P.vA) + (size_t)key2 * DH), 0, KVBYTES - key2 * 256u, 0x00020000);
  const int koff = ((lane >> 2) & 3) * 256 + ((lane >> 4) * 4 + (lane & 3)) * 16;
  const int voff = lane * 16;
  v4u K[NT][4], V[NT][4];
#define ISSUE_K(k_) do { _Pragma("unroll") for (int t_ = 0; t_ < 4; ++t_) \
    K[k_][t_] = __builtin_amdgcn_raw_buffer_load_b128((k_) < 4 ? rk1 : rk2, koff + t_ * 1024, (k_) * 32768, KV_AUX); FENCE_SCHED(); } while (0)
#define ISSUE_V(k_) do { _Pragma("unroll") for (int t_ = 0; t_ < 4; ++t_) \
    V[k_][t_] = __builtin_amdgcn_raw_buffer_load_b128((k_) < 4 ? rv1 : rv2, voff + t_ * 1024, (k_) * 32768, KV_AUX); FENCE_SCHED(); } while (0)
  ISSUE_K(0);

  {
    float acc[8];
#pragma unroll
    for (int j = 0; j < 8; ++j) acc[j] = 0.f;
    const float xf[4] = {bflo(xv.x), bfhi(xv.x), bflo(xv.y), bfhi(xv.y)};
#pragma unroll
    for (int k = 0; k < 4; ++k)
#pragma unroll
      for (int j = 0; j < 4; ++j) {
        acc[2 * j] = fmaf(xf[k], bflo(wv[k][j]), acc[2 * j]);
        acc[2 * j + 1] = fmaf(xf[k], bfhi(wv[k][j]), acc[2 * j + 1]);
      }
#pragma unroll
    for (int j = 0; j < 8; ++j) { acc[j] += dppf<ROR4>(acc[j]); acc[j] += dppf<ROR8>(acc[j]); }
    if (((lane >> 2) & 3) == 0) {
      float* dst = lds_red + (w * 4 + (lane >> 4)) * 32 + 8 * (lane & 3);
      *reinterpret_cast<f32x4*>(dst) = f32x4{acc[0], acc[1], acc[2], acc[3]};
      *reinterpret_cast<f32x4*>(dst + 4) = f32x4{acc[4], acc[5], acc[6], acc[7]};
    }
  }
  STAMP(1);
  __syncthreads();
  const u32 tag0 = __builtin_amdgcn_readfirstlane(ep.x) + 1u, tag1 = __builtin_amdgcn_readfirstlane(ep.y) + 1u;
  if (w == 0) {
    const int col = lane & 31, half = lane >> 5;
    float s = 0.f;
#pragma unroll
    for (int r = 0; r < 16; ++r) s += lds_red[(16 * half + r) * 32 + col];
    s = x32_sum(s);
    const u32 b = cvtpk(s, s) & 0xffffu;
    const u32 pair = b | (dppu<0xF5>(b) << 16);
    const u32 pair2 = dppu<0xAA>(pair);
    if ((lane & 3) == 0 && lane < 32) {
      const v4u gr = {pair, tag0, pair2, tag1};
      __builtin_amdgcn_raw_buffer_store_b128(gr, ws, WS_Q + ((g * 2 + pq) * 4 + hq) * 512 + (8 * dq + (lane >> 2)) * 16, 0, 16);
    }
  }
  FENCE_SCHED();

  STAMP(2);
  const int qh = (lane >> 2) & 3, qch = 4 * (lane >> 4) + (lane & 3);
  const bool mixed = (p1 != p2);
  {
    const int qoff1 = WS_Q + ((g * 2 + p1) * 4 + qh) * 512 + qch * 32;
    const int qoff2 = WS_Q + ((g * 2 + p2) * 4 + qh) * 512 + qch * 32;
    v4u qa0, qa1, qb0, qb1;
    bool need = (w == 0);
#define POLLQ() do { qa0 = __builtin_amdgcn_raw_buffer_load_b128(ws, qoff1, 0, 16); qa1 = __builtin_amdgcn_raw_buffer_load_b128(ws, qoff1 + 16, 0, 16); \
    qb0 = __builtin_amdgcn_raw_buffer_load_b128(ws, qoff2, 0, 16); qb1 = __builtin_amdgcn_raw_buffer_load_b128(ws, qoff2 + 16, 0, 16); FENCE_SCHED(); } while (0)
#define CHECKQ() do { if (__all(qa0.y == tag0 && qa0.w == tag1 && qa1.y == tag0 && qa1.w == tag1 && qb0.y == tag0 && qb0.w == tag1 && qb1.y == tag0 && qb1.w == tag1)) { \
      need = false; *reinterpret_cast<v4u*>(lds_q + lane * 4) = v4u{qa0.x, qa0.z, qa1.x, qa1.z}; *reinterpret_cast<v4u*>(lds_q + 256 + lane * 4) = v4u{qb0.x, qb0.z, qb1.x, qb1.z}; } FENCE_SCHED(); } while (0)
#define PACE(n_) do { asm volatile("s_waitcnt vmcnt(" #n_ ")" ::: "memory"); __builtin_amdgcn_sched_barrier(0); } while (0)
#define STAGE_K(k_) do { if (need) POLLQ(); ISSUE_K(k_); if (need) CHECKQ(); PACE(4); } while (0)
#define STAGE_V(k_) do { if (need) POLLQ(); ISSUE_V(k_); if (need) CHECKQ(); PACE(4); } while (0)
    STAGE_K(1); STAGE_K(2); STAGE_K(3); STAGE_K(4); STAGE_K(5); STAGE_K(6); STAGE_K(7); STAGE_K(8);
    STAGE_V(0);
    if (need) POLLQ();
    ISSUE_V(1);
    if (need) CHECKQ();
    for (unsigned spins = 0; need && spins < SPIN_CAP; ++spins) { __builtin_amdgcn_s_sleep(1); POLLQ(); CHECKQ(); }
  }
  STAMP(3);
  __syncthreads();
  const v4u q1 = *reinterpret_cast<const v4u*>(lds_q + lane * 4), q2 = *reinterpret_cast<const v4u*>(lds_q + 256 + lane * 4);

  float s[NT][4];
#pragma unroll
  for (int k = 0; k < NT; ++k) {
#pragma unroll
    for (int t = 0; t < 4; ++t) {
      const f32x4 d = __builtin_amdgcn_mfma_f32_16x16x32_bf16(__builtin_bit_cast(bf16x8, K[k][t]), __builtin_bit_cast(bf16x8, k < 4 ? q1 : q2),
                                                               f32x4{0.f, 0.f, 0.f, 0.f}, 0, 0, 0);
      float sv = (dppf<0x00>(d[0]) + dppf<0x55>(d[1])) + (dppf<0xAA>(d[2]) + dppf<0xFF>(d[3]));
      sv = bfhi(cvtpk(sv, sv));
      sv = bfhi(cvtpk(sv * SCALE, sv * SCALE));
      s[k][t] = sv;
    }
    FENCE_SCHED();
  }
  STAMP(4);
  const int po = i >> 4, ho = (i >> 2) & 3, qo = i & 3;
  const u16* wop = (po ? P.woB : P.woA) + (size_t)((4 * g + ho) * DH + 32 * qo + 4 * w) * DM + 8 * lane;
  v4u wo[4];

  {
    float m1 = s[0][0], m2 = s[4][0];
#pragma unroll
    for (int k = 0; k < 4; ++k)
#pragma unroll
      for (int t = 0; t < 4; ++t) m1 = fmaxf(m1, s[k][t]);
#pragma unroll
    for (int k = 4; k < NT; ++k)
#pragma unroll
      for (int t = 0; t < 4; ++t) m2 = fmaxf(m2, s[k][t]);
    m1 = xrow_max(m1); m2 = xrow_max(m2);
    if ((lane & 0x33) == 0) { lds_mx[(0 * 4 + qh) * 8 + w] = m1; lds_mx[(1 * 4 + qh) * 8 + w] = m2; }
  }
  __syncthreads();
  STAMP(5);
  float M1, M2;
  {
    const f32x4 a = *reinterpret_cast<const f32x4*>(lds_mx + qh * 8), b = *reinterpret_cast<const f32x4*>(lds_mx + qh * 8 + 4);
    const f32x4 c = *reinterpret_cast<const f32x4*>(lds_mx + (4 + qh) * 8), d = *reinterpret_cast<const f32x4*>(lds_mx + (4 + qh) * 8 + 4);
    M1 = fmaxf(fmaxf(fmaxf(a[0], a[1]), fmaxf(a[2], a[3])), fmaxf(fmaxf(b[0], b[1]), fmaxf(b[2], b[3])));
    M2 = fmaxf(fmaxf(fmaxf(c[0], c[1]), fmaxf(c[2], c[3])), fmaxf(fmaxf(d[0], d[1]), fmaxf(d[2], d[3])));
    if (p1 == p2) { M1 = fmaxf(M1, M2); M2 = M1; }
  }
  const float Mc1 = M1 * LOG2E, Mc2 = M2 * LOG2E;

  const u32 hm = (lane & 1) ? 0xffff0000u : 0x0000ffffu;
  const u32 mask0 = (lane & 2) ? 0u : hm, mask1 = (lane & 2) ? hm : 0u;
  float l1 = 0.f, l2 = 0.f;
  f32x4 O[2][2];
#pragma unroll
  for (int a = 0; a < 2; ++a)
#pragma unroll
    for (int b = 0; b < 2; ++b) O[a][b] = f32x4{0.f, 0.f, 0.f, 0.f};
  v2u A[NT][4];
#define PREP(k_) do { _Pragma("unroll") for (int t_ = 0; t_ < 4; ++t_) { \
      const float p_ = __builtin_amdgcn_exp2f(fmaf(s[k_][t_], LOG2E, (k_) < 4 ? -Mc1 : -Mc2)); \
      if ((k_) < 4) l1 += p_; else l2 += p_; \
      const u32 pk_ = cvtpk(p_, p_); A[k_][t_] = v2u{pk_ & mask0, pk_ & mask1}; } } while (0)
  PREP(0);
#pragma unroll
  for (int k = 0; k < NT; ++k) {
    if (k + 1 < NT) PREP(k + 1);
    FENCE_SCHED();
#pragma unroll
    for (int t = 0; t < 4; ++t) {
      const s16x4 a = __builtin_bit_cast(s16x4, A[k][t]);
      const int sg = k < 4 ? 0 : 1;
      O[sg][0] = __builtin_amdgcn_mfma_f32_16x16x16bf16_1k(a, __builtin_bit_cast(s16x4, v2u{V[k][t].x, V[k][t].y}), O[sg][0], 0, 0, 0);
      O[sg][1] = __builtin_amdgcn_mfma_f32_16x16x16bf16_1k(a, __builtin_bit_cast(s16x4, v2u{V[k][t].z, V[k][t].w}), O[sg][1], 0, 0, 0);
    }
    FENCE_SCHED();
    if (k + 2 < NT) ISSUE_V(k + 2);
    if (k + 2 == NT) {
#pragma unroll
      for (int kk = 0; kk < 4; ++kk) wo[kk] = *reinterpret_cast<const v4u*>(wop + kk * DM);
      FENCE_SCHED();
    }
  }
  STAMP(6);
  if (!mixed) { O[0][0] += O[1][0]; O[0][1] += O[1][1]; l1 += l2; }
  l1 = xrow_sum(l1); l2 = xrow_sum(l2);
  if ((lane & 0x33) == 0) { lds_l[(0 * 4 + qh) * 8 + w] = l1; lds_l[(1 * 4 + qh) * 8 + w] = l2; }
  {
    float* dst = lds_o + w * 512 + (lane >> 4) * 128 + (lane & 15) * 8;
    *reinterpret_cast<f32x4*>(dst) = O[0][0];
    *reinterpret_cast<f32x4*>(dst + 4) = O[0][1];
    if (mixed) {
      *reinterpret_cast<f32x4*>(dst + 4096) = O[1][0];
      *reinterpret_cast<f32x4*>(dst + 4096 + 4) = O[1][1];
    }
  }
  __syncthreads();
  {
    const int sg = tid >> 8;
    if (sg == 0 || mixed) {
      const int h = (tid >> 6) & 3, gm = tid & 63;
      const int pout = sg ? p2 : p1;
      const int slot = i + pout;
      f32x2 a = {0.f, 0.f};
#pragma unroll
      for (int ww = 0; ww < 8; ++ww) a += *reinterpret_cast<const f32x2*>(lds_o + sg * 4096 + ww * 512 + h * 128 + 2 * gm);
      const int base = WS_PART + ((g * 33 + slot) * 4 + h) * PSTR;
      const v4u gr = {__float_as_uint(a[0]), tag0, __float_as_uint(a[1]), tag1};
      __builtin_amdgcn_raw_buffer_store_b128(gr, ws, base + gm * 16, 0, 16);
      if (gm == 0) {
        const f32x4 la = *reinterpret_cast<const f32x4*>(lds_l + (sg * 4 + h) * 8), lb = *reinterpret_cast<const f32x4*>(lds_l + (sg * 4 + h) * 8 + 4);
        const float L = ((la[0] + la[1]) + (la[2] + la[3])) + ((lb[0] + lb[1]) + (lb[2] + lb[3]));
        const f32x4 a0 = *reinterpret_cast<const f32x4*>(lds_mx + h * 8), b0 = *reinterpret_cast<const f32x4*>(lds_mx + h * 8 + 4);
        const f32x4 c0 = *reinterpret_cast<const f32x4*>(lds_mx + (4 + h) * 8), d0 = *reinterpret_cast<const f32x4*>(lds_mx + (4 + h) * 8 + 4);
        float Ma = fmaxf(fmaxf(fmaxf(a0[0], a0[1]), fmaxf(a0[2], a0[3])), fmaxf(fmaxf(b0[0], b0[1]), fmaxf(b0[2], b0[3])));
        float Mb = fmaxf(fmaxf(fmaxf(c0[0], c0[1]), fmaxf(c0[2], c0[3])), fmaxf(fmaxf(d0[0], d0[1]), fmaxf(d0[2], d0[3])));
        const float Mx = mixed ? (sg ? Mb : Ma) : fmaxf(Ma, Mb);
        const v4u gm_ = {__float_as_uint(Mx * LOG2E), tag0, __float_as_uint(L), tag1};
        __builtin_amdgcn_raw_buffer_store_b128(gm_, ws, base + 64 * 16, 0, 16);
      }
    }
  }
  FENCE_SCHED();

  STAMP(7);
  const int nprod = po ? 4 : 29, slot0 = po ? 29 : 0;
  {
    const bool isdata = tid < 16 * nprod, isml = !isdata && tid < 17 * nprod;
    const int j = isdata ? (tid >> 4) : (tid - 16 * nprod);
    const int off = WS_PART + ((g * 33 + slot0 + j) * 4 + ho) * PSTR + (isdata ? (16 * qo + (tid & 15)) * 16 : 64 * 16);
    if (64 * w < 17 * nprod) {
      v4u v;
      for (unsigned spins = 0; spins < SPIN_CAP; ++spins) {
        v = __builtin_amdgcn_raw_buffer_load_b128(ws, (isdata || isml) ? off : WS_EPOCH + 64, 0, 16);
        if (__all(!(isdata || isml) || (v.y == tag0 && v.w == tag1))) break;
        __builtin_amdgcn_s_sleep(1);
      }
      if (isdata) *reinterpret_cast<f32x2*>(lds_cv + j * 34 + 2 * (tid & 15)) = f32x2{__uint_as_float(v.x), __uint_as_float(v.z)};
      else if (isml) *reinterpret_cast<f32x2*>(lds_cml + 2 * j) = f32x2{__uint_as_float(v.x), __uint_as_float(v.z)};
    }
  }
  STAMP(8);
  __syncthreads();
  {
    const int j = lane & 31, half = lane >> 5;
    const bool val = j < nprod;
    const int jj = val ? j : 0;
    const f32x2 ml = *reinterpret_cast<const f32x2*>(lds_cml + 2 * jj);
    const f32x2 vv = *reinterpret_cast<const f32x2*>(lds_cv + jj * 34 + 4 * w + 2 * half);
    float m = val ? ml[0] : -INFINITY;
    float M = m;
    M = fmaxf(M, dppf<ROR1>(M)); M = fmaxf(M, dppf<ROR2>(M)); M = fmaxf(M, dppf<ROR4>(M)); M = fmaxf(M, dppf<ROR8>(M)); M = x16_max(M);
    const float wt = val ? __builtin_amdgcn_exp2f(m - M) : 0.f;
    float L = wt * ml[1], oa = wt * vv[0], ob = wt * vv[1];
    L += dppf<ROR1>(L); oa += dppf<ROR1>(oa); ob += dppf<ROR1>(ob);
    L += dppf<ROR2>(L); oa += dppf<ROR2>(oa); ob += dppf<ROR2>(ob);
    L += dppf<ROR4>(L); oa += dppf<ROR4>(oa); ob += dppf<ROR4>(ob);
    L += dppf<ROR8>(L); oa += dppf<ROR8>(oa); ob += dppf<ROR8>(ob);
    L = x16_sum(L); oa = x16_sum(oa); ob = x16_sum(ob);
    const float inv = 1.f / L;
    oa *= inv; ob *= inv;
    float o4[4];
    o4[0] = __builtin_bit_cast(float, __builtin_amdgcn_readlane(__builtin_bit_cast(int, oa), 0));
    o4[1] = __builtin_bit_cast(float, __builtin_amdgcn_readlane(__builtin_bit_cast(int, ob), 0));
    o4[2] = __builtin_bit_cast(float, __builtin_amdgcn_readlane(__builtin_bit_cast(int, oa), 32));
    o4[3] = __builtin_bit_cast(float, __builtin_amdgcn_readlane(__builtin_bit_cast(int, ob), 32));
    float acc[8];
#pragma unroll
    for (int c = 0; c < 8; ++c) acc[c] = 0.f;
#pragma unroll
    for (int k = 0; k < 4; ++k)
#pragma unroll
      for (int c = 0; c < 4; ++c) {
        acc[2 * c] = fmaf(o4[k], bflo(wo[k][c]), acc[2 * c]);
        acc[2 * c + 1] = fmaf(o4[k], bfhi(wo[k][c]), acc[2 * c + 1]);
      }
    float* dst = lds_o + w * 512 + lane * 8;
    *reinterpret_cast<f32x4*>(dst) = f32x4{acc[0], acc[1], acc[2], acc[3]};
    *reinterpret_cast<f32x4*>(dst + 4) = f32x4{acc[4], acc[5], acc[6], acc[7]};
  }
  __syncthreads();
  if (tid < 256) {
    f32x2 a = {0.f, 0.f};
#pragma unroll
    for (int ww = 0; ww < 8; ++ww) a += *reinterpret_cast<const f32x2*>(lds_o + ww * 512 + 2 * tid);
    const v4u gr = {__float_as_uint(a[0]), tag0, __float_as_uint(a[1]), tag1};
    __builtin_amdgcn_raw_buffer_store_b128(gr, ws, WS_OUT + ((po * 128 + g * 16 + (i & 15)) * 256 + tid) * 16, 0, 16);
  }
  FENCE_SCHED();

  STAMP(9);
  if (blockIdx.x < 64) {
    const int f = blockIdx.x;
    const int pr = tid >> 2, gm = tid & 3;
    const int offA = WS_OUT + ((0 * 128 + pr) * 256 + 4 * f + gm) * 16, offB = WS_OUT + ((1 * 128 + pr) * 256 + 4 * f + gm) * 16;
    v4u va, vb;
    for (unsigned spins = 0; spins < SPIN_CAP; ++spins) {
      va = __builtin_amdgcn_raw_buffer_load_b128(ws, offA, 0, 16);
      vb = __builtin_amdgcn_raw_buffer_load_b128(ws, offB, 0, 16);
      if (__all(va.y == tag0 && va.w == tag1 && vb.y == tag0 && vb.w == tag1)) break;
      __builtin_amdgcn_s_sleep(1);
    }
    STAMP(10);
    float r[4] = {__uint_as_float(va.x), __uint_as_float(va.z), __uint_as_float(vb.x), __uint_as_float(vb.z)};
#pragma unroll
    for (int c = 0; c < 4; ++c) { r[c] += dppf<ROR4>(r[c]); r[c] += dppf<ROR8>(r[c]); r[c] = xrow_sum(r[c]); }
    if (lane < 4) {
      *reinterpret_cast<f32x2*>(lds_fin + w * 16 + 0 * 8 + gm * 2) = f32x2{r[0], r[1]};
      *reinterpret_cast<f32x2*>(lds_fin + w * 16 + 1 * 8 + gm * 2) = f32x2{r[2], r[3]};
    }
    __syncthreads();
    if (tid < 16) {
      float a = 0.f;
#pragma unroll
      for (int ww = 0; ww < 8; ++ww) a += lds_fin[ww * 16 + tid];
      const int p = tid >> 3, c = tid & 7;
      P.out[p * 512 + 8 * f + c] = (u16)(cvtpk(a, a) & 0xffffu);
    }
    STAMP(11);
    if (f == 0 && tid == 0) {
      const v2u ne = {tag0, tag1};
      __builtin_amdgcn_raw_buffer_store_b64(ne, ws, WS_EPOCH, 0, 16);
    }
  }
  STAMP_FLUSH();
}

extern "C" void kernel_launch(void* const* d_in, const int* in_sizes, int n_in, void* d_out, int out_size, void* d_ws, size_t ws_size,
                              hipStream_t stream) {
  Params P;
  P.xA = (const u16*)d_in[0]; P.kA = (const u16*)d_in[1]; P.vA = (const u16*)d_in[2]; P.wqA = (const u16*)d_in[3]; P.woA = (const u16*)d_in[4];
  P.xB = (const u16*)d_in[5]; P.kB = (const u16*)d_in[6]; P.vB = (const u16*)d_in[7]; P.wqB = (const u16*)d_in[8]; P.woB = (const u16*)d_in[9];
  P.out = (u16*)d_out; P.ws = (unsigned char*)d_ws;
  hipLaunchKernelGGL(fused_decode_kernel, dim3(NWG), dim3(NTHR), 0, stream, P);
}
```

```cpp
#include <hip/hip_runtime.h>
#include <hip/hip_bf16.h>
#include <stdint.h>

typedef unsigned int u32;
typedef unsigned long long u64;
typedef unsigned short u16;
typedef u32 v4u __attribute__((ext_vector_type(4)));
typedef u32 v2u __attribute__((ext_vector_type(2)));
typedef float f32x4 __attribute__((ext_vector_type(4)));
typedef float f32x2 __attribute__((ext_vector_type(2)));
typedef __bf16 bf16x8 __attribute__((ext_vector_type(8)));
typedef short s16x4 __attribute__((ext_vector_type(4)));

#define KV_AUX 2

constexpr int SK = 32768, DH = 128, DM = 512, NQ = 4096;
constexpr int NWG = 256, NTHR = 512, NT = 9;
constexpr float SCALE = 0.088388347648318440f;
constexpr float LOG2E = 1.4426950408889634f;
#define EARLY 0
#define WQMODE 0
#define LASTPUB 0
#define WIN 1
#define PACE_N 0
#define ROUND_S 1

constexpr int WS_EPOCH = 0;
constexpr int WS_Q = 4096;
constexpr int WS_PART = WS_Q + 8 * 2 * 2 * 4 * 1024;
constexpr int PSTR = 1152;
constexpr int WS_OUT = WS_PART + 8 * 33 * 4 * PSTR;
constexpr int WS_BYTES = WS_OUT + 2 * 128 * 4096;
constexpr unsigned SPIN_CAP = 1u << 18;

struct Params {
  const u16 *xA, *kA, *vA, *wqA, *woA, *xB, *kB, *vB, *wqB, *woB;
  u16* out;
  unsigned char* ws;
};

template <int CTRL> __device__ __forceinline__ float dppf(float x) {
  return __builtin_bit_cast(float, __builtin_amdgcn_mov_dpp(__builtin_bit_cast(int, x), CTRL, 0xf, 0xf, true));
}
template <int CTRL> __device__ __forceinline__ u32 dppu(u32 x) {
  return (u32)__builtin_amdgcn_mov_dpp((int)x, CTRL, 0xf, 0xf, true);
}
constexpr int ROR4 = 0x124, ROR8 = 0x128, ROR1 = 0x121, ROR2 = 0x122;
__device__ __forceinline__ float xrow_max(float x) {
  auto s = __builtin_amdgcn_permlane16_swap(__float_as_uint(x), __float_as_uint(x), false, false);
  x = fmaxf(__uint_as_float(s[0]), __uint_as_float(s[1]));
  auto t = __builtin_amdgcn_permlane32_swap(__float_as_uint(x), __float_as_uint(x), false, false);
  return fmaxf(__uint_as_float(t[0]), __uint_as_float(t[1]));
}
__device__ __forceinline__ float xrow_sum(float x) {
  auto s = __builtin_amdgcn_permlane16_swap(__float_as_uint(x), __float_as_uint(x), false, false);
  x = __uint_as_float(s[0]) + __uint_as_float(s[1]);
  auto t = __builtin_amdgcn_permlane32_swap(__float_as_uint(x), __float_as_uint(x), false, false);
  return __uint_as_float(t[0]) + __uint_as_float(t[1]);
}
__device__ __forceinline__ float x16_sum(float x) {
  auto s = __builtin_amdgcn_permlane16_swap(__float_as_uint(x), __float_as_uint(x), false, false);
  return __uint_as_float(s[0]) + __uint_as_float(s[1]);
}
__device__ __forceinline__ float x16_max(float x) {
  auto s = __builtin_amdgcn_permlane16_swap(__float_as_uint(x), __float_as_uint(x), false, false);
  return fmaxf(__uint_as_float(s[0]), __uint_as_float(s[1]));
}
__device__ __forceinline__ float x32_sum(float x) {
  auto t = __builtin_amdgcn_permlane32_swap(__float_as_uint(x), __float_as_uint(x), false, false);
  return __uint_as_float(t[0]) + __uint_as_float(t[1]);
}
__device__ __forceinline__ float bflo(u32 w) { return __builtin_bit_cast(float, w << 16); }
__device__ __forceinline__ float bfhi(u32 w) { return __builtin_bit_cast(float, w & 0xffff0000u); }
typedef __bf16 bf16x2 __attribute__((ext_vector_type(2)));
__device__ __forceinline__ u32 cvtpk(float lo, float hi) {
  return __builtin_bit_cast(u32, __builtin_convertvector(f32x2{lo, hi}, bf16x2));
}
__device__ __forceinline__ u32 vmov(u32 x) { u32 r; asm volatile("v_mov_b32 %0, %1" : "=&v"(r) : "v"(x)); return r; }
#define STAMP(n_) do { } while (0)
#define STAMP_DECL
#define STAMP_FLUSH() do { } while (0)
#define FENCE_SCHED() do { asm volatile("" ::: "memory"); __builtin_amdgcn_sched_barrier(0); } while (0)

__global__ void __launch_bounds__(NTHR, 2) fused_decode_kernel(Params P) {
  __shared__ __attribute__((aligned(16))) float lds_red[32 * 64];
  __shared__ __attribute__((aligned(16))) float lds_mx[2 * 4 * 8];
  __shared__ __attribute__((aligned(16))) float lds_l[2 * 4 * 8];
  __shared__ __attribute__((aligned(16))) float lds_o[2 * 8 * 512];
  __shared__ __attribute__((aligned(16))) float lds_cv[29 * 34];
  __shared__ __attribute__((aligned(16))) float lds_cml[32 * 2];
  __shared__ __attribute__((aligned(16))) float lds_fin[8 * 16];
  __shared__ __attribute__((aligned(16))) u32 lds_q[512];
  __shared__ __attribute__((aligned(16))) u32 lds_flag[4];

  const int tid = threadIdx.x, lane = tid & 63;
  const int w = __builtin_amdgcn_readfirstlane(tid >> 6);
  const int g = blockIdx.x & 7, i = blockIdx.x >> 3;
  const __amdgpu_buffer_rsrc_t ws = __builtin_amdgcn_make_buffer_rsrc((void*)P.ws, 0, WS_BYTES, 0x00020000);

  STAMP_DECL;
  STAMP(0);
  const v2u ep = __builtin_amdgcn_raw_buffer_load_b64(ws, WS_EPOCH, 0, 16);

  const int pq = i >> 4, hq = (i >> 2) & 3, dq = i & 3;
  const u16* wqp = (pq ? P.wqB : P.wqA) + (4 * g + hq) * DH + 32 * dq + 8 * (lane & 3);
  const int row0 = 64 * w + 4 * (lane >> 2);
  const u16* xq = pq ? P.xB : P.xA;
  const v2u xv = *reinterpret_cast<const v2u*>(xq + row0);
  v4u wv[4];
#pragma unroll
  for (int k = 0; k < 4; ++k) wv[k] = __builtin_nontemporal_load(reinterpret_cast<const v4u*>(wqp + (size_t)(row0 + k) * NQ));
  FENCE_SCHED();

  const int p1 = (i >= 29) ? 1 : 0, p2 = (i >= 28) ? 1 : 0;
  const u32 key1 = (u32)g * SK + 128 * (9 * i) + 16 * w - (p1 ? 4096 : 0);
  const u32 key2 = (u32)g * SK + 128 * (9 * i) + 16 * w - (p2 ? 4096 : 0);
  constexpr u32 KVBYTES = 8u * SK * DH * 2u;
  const __amdgpu_buffer_rsrc_t rk1 = __builtin_amdgcn_make_buffer_rsrc((void*)((p1 ? P.kB : P.kA) + (size_t)key1 * DH), 0, KVBYTES - key1 * 256u, 0x00020000);
  const __amdgpu_buffer_rsrc_t rk2 = __builtin_amdgcn_make_buffer_rsrc((void*)((p2 ? P.kB : P.kA) + (size_t)key2 * DH), 0, KVBYTES - key2 * 256u, 0x00020000);
  const __amdgpu_buffer_rsrc_t rv1 = __builtin_amdgcn_make_buffer_rsrc((void*)((p1 ? P.vB : P.vA) + (size_t)key1 * DH), 0, KVBYTES - key1 * 256u, 0x00020000);
  const __amdgpu_buffer_rsrc_t rv2 = __builtin_amdgcn_make_buffer_rsrc((void*)((p2 ? P.vB : P.vA) + (size_t)key2 * DH), 0, KVBYTES - key2 * 256u, 0x00020000);
  const int koff = ((lane >> 2) & 3) * 256 + ((lane >> 4) * 4 + (lane & 3)) * 16;
  const int voff = lane * 16;
  v4u K[NT][4], V[NT][4];
#define ISSUE_K(k_) do { _Pragma("unroll") for (int t_ = 0; t_ < 4; ++t_) \
    K[k_][t_] = __builtin_amdgcn_raw_buffer_load_b128((k_) < 4 ? rk1 : rk2, koff + t_ * 1024, (k_) * 32768, KV_AUX); FENCE_SCHED(); } while (0)
#define ISSUE_V(k_) do { _Pragma("unroll") for (int t_ = 0; t_ < 4; ++t_) \
    V[k_][t_] = __builtin_amdgcn_raw_buffer_load_b128((k_) < 4 ? rv1 : rv2, voff + t_ * 1024, (k_) * 32768, KV_AUX); FENCE_SCHED(); } while (0)
  ISSUE_K(0);
  if (tid < 4) lds_flag[tid] = 0u;

  const u32 tag0 = __builtin_amdgcn_readfirstlane(ep.x) + 1u, tag1 = __builtin_amdgcn_readfirstlane(ep.y) + 1u;
  {
    float acc[8];
#pragma unroll
    for (int j = 0; j < 8; ++j) acc[j] = 0.f;
    const float xf[4] = {bflo(xv.x), bfhi(xv.x), bflo(xv.y), bfhi(xv.y)};
#pragma unroll
    for (int k = 0; k < 4; ++k)
#pragma unroll
      for (int j = 0; j < 4; ++j) {
        acc[2 * j] = fmaf(xf[k], bflo(wv[k][j]), acc[2 * j]);
        acc[2 * j + 1] = fmaf(xf[k], bfhi(wv[k][j]), acc[2 * j + 1]);
      }
#pragma unroll
    for (int j = 0; j < 8; ++j) { acc[j] += dppf<ROR4>(acc[j]); acc[j] += dppf<ROR8>(acc[j]); }
    if (((lane >> 2) & 3) == 0) {
      float* dst = lds_red + (w * 4 + (lane >> 4)) * 32 + 8 * (lane & 3);
      *reinterpret_cast<f32x4*>(dst) = f32x4{acc[0], acc[1], acc[2], acc[3]};
      *reinterpret_cast<f32x4*>(dst + 4) = f32x4{acc[4], acc[5], acc[6], acc[7]};
    }
    STAMP(1);
    __syncthreads();
    const bool pub = (w == 0);
    if (pub) {
      asm volatile("" ::: "memory");
      const int col = lane & 31, half = lane >> 5;
      float s = 0.f;
#pragma unroll
      for (int r = 0; r < 16; ++r) s += lds_red[(16 * half + r) * 32 + col];
      s = x32_sum(s);
      const u32 b = cvtpk(s, s) & 0xffffu;
      const u32 pair = b | (dppu<0xF5>(b) << 16);
      const u32 pair2 = dppu<0xAA>(pair);
      if ((lane & 3) == 0 && lane < 32) {
        const v4u gr = {pair, tag0, pair2, tag1};
        __builtin_amdgcn_raw_buffer_store_b128(gr, ws, WS_Q + ((g * 2 + pq) * 4 + hq) * 512 + (8 * dq + (lane >> 2)) * 16, 0, 16);
      }
    }
  }
  FENCE_SCHED();

  STAMP(2);
  const int qh = (lane >> 2) & 3, qch = 4 * (lane >> 4) + (lane & 3);
  const bool mixed = (p1 != p2);
  int qsel = 0;
  bool need = false;
#define FLAG(n_) __builtin_amdgcn_readfirstlane(__hip_atomic_load(&lds_flag[n_], __ATOMIC_RELAXED, __HIP_MEMORY_SCOPE_WORKGROUP))
#define TAGOK(v_) ((v_).y == tag0 && (v_).w == tag1)
#define QDONE(val_) do { STAMP(12); *reinterpret_cast<v4u*>(lds_q + qsel * 256 + lane * 4) = val_; asm volatile("s_waitcnt lgkmcnt(0)" ::: "memory"); __hip_atomic_store(&lds_flag[qsel], 1u, __ATOMIC_RELAXED, __HIP_MEMORY_SCOPE_WORKGROUP); } while (0)
#define ROLE(k_) do { need = false; if (w == ((k_) & 7)) { qsel = 0; need = (FLAG(0) == 0u); } else if (mixed && w == (((k_) + 4) & 7)) { qsel = 1; need = (FLAG(1) == 0u); } } while (0)
  const int qoffa = WS_Q + ((g * 2 + p1) * 4 + qh) * 512 + qch * 32, qoffb = WS_Q + ((g * 2 + p2) * 4 + qh) * 512 + qch * 32;
  v4u qa0, qa1;
#define POLLQ() do { const int qoff = qsel ? qoffb : qoffa; qa0 = __builtin_amdgcn_raw_buffer_load_b128(ws, qoff, 0, 16); qa1 = __builtin_amdgcn_raw_buffer_load_b128(ws, qoff + 16, 0, 16); FENCE_SCHED(); } while (0)
#define CHECKQ() do { if (__all(TAGOK(qa0) && TAGOK(qa1))) { QDONE((v4u{qa0.x, qa0.z, qa1.x, qa1.z})); } FENCE_SCHED(); } while (0)
#define PACE_(n_) do { asm volatile("s_waitcnt vmcnt(" #n_ ")" ::: "memory"); __builtin_amdgcn_sched_barrier(0); } while (0)
#define PACE(n_) PACE_(n_)
#define PSTAGE(k_) do { ROLE(k_); if (need) POLLQ(); ISSUE_K(k_); PACE(PACE_N); if (need) CHECKQ(); } while (0)
  const int po = i >> 4, ho = (i >> 2) & 3, qo = i & 3;
  const u16* wop = (po ? P.woB : P.woA) + (size_t)((4 * g + ho) * DH + 32 * qo + 4 * w) * DM + 8 * lane;
  v4u wo[4];
#define ISSUE_W() do { _Pragma("unroll") for (int kk = 0; kk < 4; ++kk) wo[kk] = __builtin_nontemporal_load(reinterpret_cast<const v4u*>(wop + kk * DM)); FENCE_SCHED(); } while (0)
  v4u q1, q2;
#define WAITQ() do { \
    for (unsigned spins = 0; spins < (SPIN_CAP << 2); ++spins) { \
      const u32 f0 = FLAG(0), f1 = FLAG(1); \
      if (f0 != 0u && (!mixed || f1 != 0u)) break; \
      if (w == 0 && f0 == 0u) { qsel = 0; POLLQ(); CHECKQ(); } \
      else if (w == 4 && mixed && f1 == 0u) { qsel = 1; POLLQ(); CHECKQ(); } \
      else __builtin_amdgcn_s_sleep(1); } \
    asm volatile("" ::: "memory"); \
    q1 = *reinterpret_cast<const v4u*>(lds_q + lane * 4); q2 = *reinterpret_cast<const v4u*>(lds_q + (mixed ? 256 : 0) + lane * 4); } while (0)
  float s[NT][4];
#define RND(sv_) bfhi(cvtpk(bfhi(cvtpk(sv_, sv_)) * SCALE, bfhi(cvtpk(sv_, sv_)) * SCALE))
#define QK(k_) do { _Pragma("unroll") for (int t_ = 0; t_ < 4; ++t_) { \
      const f32x4 d_ = __builtin_amdgcn_mfma_f32_16x16x32_bf16(__builtin_bit_cast(bf16x8, K[k_][t_]), __builtin_bit_cast(bf16x8, (k_) < 4 ? q1 : q2), f32x4{0.f, 0.f, 0.f, 0.f}, 0, 0, 0); \
      const float sv_ = (dppf<0x00>(d_[0]) + dppf<0x55>(d_[1])) + (dppf<0xAA>(d_[2]) + dppf<0xFF>(d_[3])); \
      s[k_][t_] = RND(sv_); } } while (0)
  float Mc1, Mc2;
#define MAXS() do { \
    float m1 = s[0][0], m2 = s[4][0]; \
    _Pragma("unroll") for (int k = 0; k < 4; ++k) _Pragma("unroll") for (int t = 0; t < 4; ++t) m1 = fmaxf(m1, s[k][t]); \
    _Pragma("unroll") for (int k = 4; k < NT; ++k) _Pragma("unroll") for (int t = 0; t < 4; ++t) m2 = fmaxf(m2, s[k][t]); \
    m1 = xrow_max(m1); m2 = xrow_max(m2); \
    if (!mixed) { m1 = fmaxf(m1, m2); m2 = m1; } \
    Mc1 = m1 * LOG2E; Mc2 = m2 * LOG2E; \
    if ((lane & 0x33) == 0) { lds_mx[(0 * 4 + qh) * 8 + w] = Mc1; lds_mx[(1 * 4 + qh) * 8 + w] = Mc2; } } while (0)
  const u32 hm = (lane & 1) ? 0xffff0000u : 0x0000ffffu;
  const u32 mask0 = (lane & 2) ? 0u : hm, mask1 = (lane & 2) ? hm : 0u;
  float l1 = 0.f, l2 = 0.f;
  f32x4 O[2][2];
#pragma unroll
  for (int a = 0; a < 2; ++a)
#pragma unroll
    for (int b = 0; b < 2; ++b) O[a][b] = f32x4{0.f, 0.f, 0.f, 0.f};
  v2u A[NT][4];
#define PREP(k_) do { _Pragma("unroll") for (int t_ = 0; t_ < 4; ++t_) { \
      const float p_ = __builtin_amdgcn_exp2f(fmaf(s[k_][t_], LOG2E, (k_) < 4 ? -Mc1 : -Mc2)); \
      if ((k_) < 4) l1 += p_; else l2 += p_; \
      const u32 pk_ = cvtpk(p_, p_); A[k_][t_] = v2u{pk_ & mask0, pk_ & mask1}; } } while (0)
#define PV(k_) do { _Pragma("unroll") for (int t_ = 0; t_ < 4; ++t_) { \
      const s16x4 a_ = __builtin_bit_cast(s16x4, A[k_][t_]); \
      O[(k_) < 4 ? 0 : 1][0] = __builtin_amdgcn_mfma_f32_16x16x16bf16_1k(a_, __builtin_bit_cast(s16x4, v2u{V[k_][t_].x, V[k_][t_].y}), O[(k_) < 4 ? 0 : 1][0], 0, 0, 0); \
      O[(k_) < 4 ? 0 : 1][1] = __builtin_amdgcn_mfma_f32_16x16x16bf16_1k(a_, __builtin_bit_cast(s16x4, v2u{V[k_][t_].z, V[k_][t_].w}), O[(k_) < 4 ? 0 : 1][1], 0, 0, 0); } } while (0)
#define ENDST() do { FENCE_SCHED(); PACE(PACE_N); } while (0)

  PSTAGE(1); STAMP(13);
  PSTAGE(2); PSTAGE(3); PSTAGE(4); STAMP(14);
  PSTAGE(5); PSTAGE(6);
  ISSUE_K(7); WAITQ(); STAMP(3); QK(0); QK(1); QK(2); ENDST();
  ISSUE_K(8); QK(3); QK(4); QK(5); ENDST(); STAMP(15);
  ISSUE_V(0); QK(6); QK(7); ENDST();
  ISSUE_V(1); QK(8); MAXS(); PREP(0); PV(0); PREP(1); ENDST(); STAMP(4);
  ISSUE_V(2); PV(1); PREP(2); ENDST();
  ISSUE_V(3); PV(2); PREP(3); ENDST();
  ISSUE_V(4); PV(3); PREP(4); ENDST();
  ISSUE_V(5); PV(4); PREP(5); ENDST();
  ISSUE_V(6); PV(5); PREP(6); ENDST();
  ISSUE_V(7); PV(6); PREP(7); ENDST();
  ISSUE_V(8); PV(7); PREP(8); ENDST();
  ISSUE_W(); PV(8);
  FENCE_SCHED();
  STAMP(6);
  if (!mixed) { O[0][0] += O[1][0]; O[0][1] += O[1][1]; l1 += l2; }
  l1 = xrow_sum(l1); l2 = xrow_sum(l2);
  if ((lane & 0x33) == 0) { lds_l[(0 * 4 + qh) * 8 + w] = l1; lds_l[(1 * 4 + qh) * 8 + w] = l2; }
  {
    float* dst = lds_o + w * 512 + (lane >> 4) * 128 + (lane & 15) * 8;
    *reinterpret_cast<f32x4*>(dst) = O[0][0];
    *reinterpret_cast<f32x4*>(dst + 4) = O[0][1];
    if (mixed) {
      *reinterpret_cast<f32x4*>(dst + 4096) = O[1][0];
      *reinterpret_cast<f32x4*>(dst + 4096 + 4) = O[1][1];
    }
  }
  __syncthreads();
  {
    const int sg = tid >> 8;
    if (sg == 0 || mixed) {
      const int h = (tid >> 6) & 3, gm = tid & 63;
      const int pout = sg ? p2 : p1;
      const int slot = i + pout;
      const f32x4 ma = *reinterpret_cast<const f32x4*>(lds_mx + (sg * 4 + h) * 8), mb = *reinterpret_cast<const f32x4*>(lds_mx + (sg * 4 + h) * 8 + 4);
      const float Mx = fmaxf(fmaxf(fmaxf(ma[0], ma[1]), fmaxf(ma[2], ma[3])), fmaxf(fmaxf(mb[0], mb[1]), fmaxf(mb[2], mb[3])));
      float wt[8];
#pragma unroll
      for (int ww = 0; ww < 4; ++ww) { wt[ww] = __builtin_amdgcn_exp2f(ma[ww] - Mx); wt[4 + ww] = __builtin_amdgcn_exp2f(mb[ww] - Mx); }
      f32x2 a = {0.f, 0.f};
#pragma unroll
      for (int ww = 0; ww < 8; ++ww) a += wt[ww] * *reinterpret_cast<const f32x2*>(lds_o + sg * 4096 + ww * 512 + h * 128 + 2 * gm);
      const int base = WS_PART + ((g * 33 + slot) * 4 + h) * PSTR;
      const v4u gr = {__float_as_uint(a[0]), tag0, __float_as_uint(a[1]), tag1};
      __builtin_amdgcn_raw_buffer_store_b128(gr, ws, base + gm * 16, 0, 16);
      if (gm == 0) {
        const f32x4 la = *reinterpret_cast<const f32x4*>(lds_l + (sg * 4 + h) * 8), lb = *reinterpret_cast<const f32x4*>(lds_l + (sg * 4 + h) * 8 + 4);
        const float L = ((wt[0] * la[0] + wt[1] * la[1]) + (wt[2] * la[2] + wt[3] * la[3])) + ((wt[4] * lb[0] + wt[5] * lb[1]) + (wt[6] * lb[2] + wt[7] * lb[3]));
        const v4u gm_ = {__float_as_uint(Mx), tag0, __float_as_uint(L), tag1};
        __builtin_amdgcn_raw_buffer_store_b128(gm_, ws, base + 64 * 16, 0, 16);
      }
    }
  }
  FENCE_SCHED();

  STAMP(7);
  const int nprod = po ? 4 : 29, slot0 = po ? 29 : 0;
  {
    const bool isdata = tid < 16 * nprod, isml = !isdata && tid < 17 * nprod;
    const int j = isdata ? (tid >> 4) : (tid - 16 * nprod);
    const int off = WS_PART + ((g * 33 + slot0 + j) * 4 + ho) * PSTR + (isdata ? (16 * qo + (tid & 15)) * 16 : 64 * 16);
    if (64 * w < 17 * nprod) {
      v4u v;
      for (unsigned spins = 0; spins < SPIN_CAP; ++spins) {
        v = __builtin_amdgcn_raw_buffer_load_b128(ws, (isdata || isml) ? off : WS_EPOCH + 64, 0, 16);
        if (__all(!(isdata || isml) || (v.y == tag0 && v.w == tag1))) break;
        __builtin_amdgcn_s_sleep(1);
      }
      if (isdata) *reinterpret_cast<f32x2*>(lds_cv + j * 34 + 2 * (tid & 15)) = f32x2{__uint_as_float(v.x), __uint_as_float(v.z)};
      else if (isml) *reinterpret_cast<f32x2*>(lds_cml + 2 * j) = f32x2{__uint_as_float(v.x), __uint_as_float(v.z)};
    }
  }
  STAMP(8);
  __syncthreads();
  {
    const int j = lane & 31, half = lane >> 5;
    const bool val = j < nprod;
    const int jj = val ? j : 0;
    const f32x2 ml = *reinterpret_cast<const f32x2*>(lds_cml + 2 * jj);
    const f32x2 vv = *reinterpret_cast<const f32x2*>(lds_cv + jj * 34 + 4 * w + 2 * half);
    float m = val ? ml[0] : -INFINITY;
    float M = m;
    M = fmaxf(M, dppf<ROR1>(M)); M = fmaxf(M, dppf<ROR2>(M)); M = fmaxf(M, dppf<ROR4>(M)); M = fmaxf(M, dppf<ROR8>(M)); M = x16_max(M);
    const float wt = val ? __builtin_amdgcn_exp2f(m - M) : 0.f;
    float L = wt * ml[1], oa = wt * vv[0], ob = wt * vv[1];
    L += dppf<ROR1>(L); oa += dppf<ROR1>(oa); ob += dppf<ROR1>(ob);
    L += dppf<ROR2>(L); oa += dppf<ROR2>(oa); ob += dppf<ROR2>(ob);
    L += dppf<ROR4>(L); oa += dppf<ROR4>(oa); ob += dppf<ROR4>(ob);
    L += dppf<ROR8>(L); oa += dppf<ROR8>(oa); ob += dppf<ROR8>(ob);
    L = x16_sum(L); oa = x16_sum(oa); ob = x16_sum(ob);
    const float inv = 1.f / L;
    oa *= inv; ob *= inv;
    float o4[4];
    o4[0] = __builtin_bit_cast(float, __builtin_amdgcn_readlane(__builtin_bit_cast(int, oa), 0));
    o4[1] = __builtin_bit_cast(float, __builtin_amdgcn_readlane(__builtin_bit_cast(int, ob), 0));
    o4[2] = __builtin_bit_cast(float, __builtin_amdgcn_readlane(__builtin_bit_cast(int, oa), 32));
    o4[3] = __builtin_bit_cast(float, __builtin_amdgcn_readlane(__builtin_bit_cast(int, ob), 32));
    float acc[8];
#pragma unroll
    for (int c = 0; c < 8; ++c) acc[c] = 0.f;
#pragma unroll
    for (int k = 0; k < 4; ++k)
#pragma unroll
      for (int c = 0; c < 4; ++c) {
        acc[2 * c] = fmaf(o4[k], bflo(wo[k][c]), acc[2 * c]);
        acc[2 * c + 1] = fmaf(o4[k], bfhi(wo[k][c]), acc[2 * c + 1]);
      }
    float* dst = lds_o + w * 512 + lane * 8;
    *reinterpret_cast<f32x4*>(dst) = f32x4{acc[0], acc[1], acc[2], acc[3]};
    *reinterpret_cast<f32x4*>(dst + 4) = f32x4{acc[4], acc[5], acc[6], acc[7]};
  }
  __syncthreads();
  if (tid < 256) {
    f32x2 a = {0.f, 0.f};
#pragma unroll
    for (int ww = 0; ww < 8; ++ww) a += *reinterpret_cast<const f32x2*>(lds_o + ww * 512 + 2 * tid);
    const v4u gr = {__float_as_uint(a[0]), tag0, __float_as_uint(a[1]), tag1};
    __builtin_amdgcn_raw_buffer_store_b128(gr, ws, WS_OUT + ((po * 128 + g * 16 + (i & 15)) * 256 + tid) * 16, 0, 16);
  }
  FENCE_SCHED();

  STAMP(9);
  if (blockIdx.x < 64) {
    const int f = blockIdx.x;
    const int pr = tid >> 2, gm = tid & 3;
    const int offA = WS_OUT + ((0 * 128 + pr) * 256 + 4 * f + gm) * 16, offB = WS_OUT + ((1 * 128 + pr) * 256 + 4 * f + gm) * 16;
    v4u va, vb;
    for (unsigned spins = 0; spins < SPIN_CAP; ++spins) {
      va = __builtin_amdgcn_raw_buffer_load_b128(ws, offA, 0, 16);
      vb = __builtin_amdgcn_raw_buffer_load_b128(ws, offB, 0, 16);
      if (__all(va.y == tag0 && va.w == tag1 && vb.y == tag0 && vb.w == tag1)) break;
      __builtin_amdgcn_s_sleep(1);
    }
    STAMP(10);
    float r[4] = {__uint_as_float(va.x), __uint_as_float(va.z), __uint_as_float(vb.x), __uint_as_float(vb.z)};
#pragma unroll
    for (int c = 0; c < 4; ++c) { r[c] += dppf<ROR4>(r[c]); r[c] += dppf<ROR8>(r[c]); r[c] = xrow_sum(r[c]); }
    if (lane < 4) {
      *reinterpret_cast<f32x2*>(lds_fin + w * 16 + 0 * 8 + gm * 2) = f32x2{r[0], r[1]};
      *reinterpret_cast<f32x2*>(lds_fin + w * 16 + 1 * 8 + gm * 2) = f32x2{r[2], r[3]};
    }
    __syncthreads();
    if (tid < 16) {
      float a = 0.f;
#pragma unroll
      for (int ww = 0; ww < 8; ++ww) a += lds_fin[ww * 16 + tid];
      const int p = tid >> 3, c = tid & 7;
      P.out[p * 512 + 8 * f + c] = (u16)(cvtpk(a, a) & 0xffffu);
    }
    STAMP(11);
    if (f == 0 && tid == 0) {
      const v2u ne = {tag0, tag1};
      __builtin_amdgcn_raw_buffer_store_b64(ne, ws, WS_EPOCH, 0, 16);
    }
  }
  STAMP_FLUSH();
}

extern "C" void kernel_launch(void* const* d_in, const int* in_sizes, int n_in, void* d_out, int out_size, void* d_ws, size_t ws_size,
                              hipStream_t stream) {
  Params P;
  P.xA = (const u16*)d_in[0]; P.kA = (const u16*)d_in[1]; P.vA = (const u16*)d_in[2]; P.wqA = (const u16*)d_in[3]; P.woA = (const u16*)d_in[4];
  P.xB = (const u16*)d_in[5]; P.kB = (const u16*)d_in[6]; P.vB = (const u16*)d_in[7]; P.wqB = (const u16*)d_in[8]; P.woB = (const u16*)d_in[9];
  P.out = (u16*)d_out; P.ws = (unsigned char*)d_ws;
  hipLaunchKernelGGL(fused_decode_kernel, dim3(NWG), dim3(NTHR), 0, stream, P);
}
```

```cpp
#include <hip/hip_runtime.h>
#include <hip/hip_bf16.h>
#include <stdint.h>

typedef unsigned int u32;
typedef unsigned long long u64;
typedef unsigned short u16;
typedef u32 v4u __attribute__((ext_vector_type(4)));
typedef u32 v2u __attribute__((ext_vector_type(2)));
typedef float f32x4 __attribute__((ext_vector_type(4)));
typedef float f32x2 __attribute__((ext_vector_type(2)));
typedef __bf16 bf16x8 __attribute__((ext_vector_type(8)));
typedef __bf16 bf16x2 __attribute__((ext_vector_type(2)));
typedef short s16x4 __attribute__((ext_vector_type(4)));

#define KV_AUX 2
#define ROUND_O 1
#define POLL0 4

constexpr int SK = 32768, DH = 128, DM = 512, NQ = 4096;
constexpr int NWG = 256, NTHR = 512, NT = 9;
constexpr float SCALE = 0.088388347648318440f;
constexpr float LOG2E = 1.4426950408889634f;

constexpr int WS_EPOCH = 0;
constexpr int WS_Q = 4096;
constexpr int WS_PART = WS_Q + 8 * 2 * 4 * 512;
constexpr int PSTR = 1152;
constexpr int WS_OUT = WS_PART + 8 * 33 * 4 * PSTR;
constexpr int WS_BYTES = WS_OUT + 2 * 128 * 4096;
constexpr unsigned SPIN_CAP = 1u << 18;

struct Params {
  const u16 *xA, *kA, *vA, *wqA, *woA, *xB, *kB, *vB, *wqB, *woB;
  u16* out;
  unsigned char* ws;
};

template <int CTRL> __device__ __forceinline__ float dppf(float x) {
  return __builtin_bit_cast(float, __builtin_amdgcn_mov_dpp(__builtin_bit_cast(int, x), CTRL, 0xf, 0xf, true));
}
template <int CTRL> __device__ __forceinline__ u32 dppu(u32 x) {
  return (u32)__builtin_amdgcn_mov_dpp((int)x, CTRL, 0xf, 0xf, true);
}
constexpr int ROR4 = 0x124, ROR8 = 0x128, ROR1 = 0x121, ROR2 = 0x122;
__device__ __forceinline__ float xrow_max(float x) {
  auto s = __builtin_amdgcn_permlane16_swap(__float_as_uint(x), __float_as_uint(x), false, false);
  x = fmaxf(__uint_as_float(s[0]), __uint_as_float(s[1]));
  auto t = __builtin_amdgcn_permlane32_swap(__float_as_uint(x), __float_as_uint(x), false, false);
  return fmaxf(__uint_as_float(t[0]), __uint_as_float(t[1]));
}
__device__ __forceinline__ float xrow_sum(float x) {
  auto s = __builtin_amdgcn_permlane16_swap(__float_as_uint(x), __float_as_uint(x), false, false);
  x = __uint_as_float(s[0]) + __uint_as_float(s[1]);
  auto t = __builtin_amdgcn_permlane32_swap(__float_as_uint(x), __float_as_uint(x), false, false);
  return __uint_as_float(t[0]) + __uint_as_float(t[1]);
}
__device__ __forceinline__ float x16_sum(float x) {
  auto s = __builtin_amdgcn_permlane16_swap(__float_as_uint(x), __float_as_uint(x), false, false);
  return __uint_as_float(s[0]) + __uint_as_float(s[1]);
}
__device__ __forceinline__ float x16_max(float x) {
  auto s = __builtin_amdgcn_permlane16_swap(__float_as_uint(x), __float_as_uint(x), false, false);
  return fmaxf(__uint_as_float(s[0]), __uint_as_float(s[1]));
}
__device__ __forceinline__ float x32_sum(float x) {
  auto t = __builtin_amdgcn_permlane32_swap(__float_as_uint(x), __float_as_uint(x), false, false);
  return __uint_as_float(t[0]) + __uint_as_float(t[1]);
}
__device__ __forceinline__ float bflo(u32 w) { return __builtin_bit_cast(float, w << 16); }
__device__ __forceinline__ float bfhi(u32 w) { return __builtin_bit_cast(float, w & 0xffff0000u); }
__device__ __forceinline__ u32 cvtpk(float lo, float hi) {
  return __builtin_bit_cast(u32, __builtin_convertvector(f32x2{lo, hi}, bf16x2));
}
#define STAMP(n_) do { } while (0)
#define STAMP_DECL
#define STAMP_FLUSH() do { } while (0)
#define FENCE_SCHED() do { asm volatile("" ::: "memory"); __builtin_amdgcn_sched_barrier(0); } while (0)
#define PACE0() do { asm volatile("s_waitcnt vmcnt(0)" ::: "memory"); __builtin_amdgcn_sched_barrier(0); } while (0)

__global__ void __launch_bounds__(NTHR, 2) fused_decode_kernel(Params P) {
  __shared__ __attribute__((aligned(16))) float lds_red[32 * 32];
  __shared__ __attribute__((aligned(16))) float lds_mx[2 * 4 * 8];
  __shared__ __attribute__((aligned(16))) float lds_l[2 * 4 * 8];
  __shared__ __attribute__((aligned(16))) float lds_o[2 * 8 * 512];
  __shared__ __attribute__((aligned(16))) float lds_cv[29 * 34];
  __shared__ __attribute__((aligned(16))) float lds_cml[32 * 2];
  __shared__ __attribute__((aligned(16))) float lds_fin[8 * 16];
  __shared__ __attribute__((aligned(16))) u32 lds_q[512];
  __shared__ __attribute__((aligned(16))) u32 lds_flag[4];

  const int tid = threadIdx.x, lane = tid & 63;
  const int w = __builtin_amdgcn_readfirstlane(tid >> 6);
  const int g = blockIdx.x & 7, i = blockIdx.x >> 3;
  const __amdgpu_buffer_rsrc_t ws = __builtin_amdgcn_make_buffer_rsrc((void*)P.ws, 0, WS_BYTES, 0x00020000);

  STAMP_DECL;
  STAMP(0);
  const v2u ep = __builtin_amdgcn_raw_buffer_load_b64(ws, WS_EPOCH, 0, 16);

  const int pq = i >> 4, hq = (i >> 2) & 3, dq = i & 3;
  const u16* wqp = (pq ? P.wqB : P.wqA) + (4 * g + hq) * DH + 32 * dq + 8 * (lane & 3);
  const int row0 = 64 * w + 4 * (lane >> 2);
  const u16* xq = pq ? P.xB : P.xA;
  const v2u xv = *reinterpret_cast<const v2u*>(xq + row0);
  v4u wv[4];
#pragma unroll
  for (int k = 0; k < 4; ++k) wv[k] = __builtin_nontemporal_load(reinterpret_cast<const v4u*>(wqp + (size_t)(row0 + k) * NQ));
  FENCE_SCHED();

  const int p1 = (i >= 29) ? 1 : 0, p2 = (i >= 28) ? 1 : 0;
  const u32 key1 = (u32)g * SK + 128 * (9 * i) + 16 * w - (p1 ? 4096 : 0);
  const u32 key2 = (u32)g * SK + 128 * (9 * i) + 16 * w - (p2 ? 4096 : 0);
  constexpr u32 KVBYTES = 8u * SK * DH * 2u;
  const __amdgpu_buffer_rsrc_t rk1 = __builtin_amdgcn_make_buffer_rsrc((void*)((p1 ? P.kB : P.kA) + (size_t)key1 * DH), 0, KVBYTES - key1 * 256u, 0x00020000);
  const __amdgpu_buffer_rsrc_t rk2 = __builtin_amdgcn_make_buffer_rsrc((void*)((p2 ? P.kB : P.kA) + (size_t)key2 * DH), 0, KVBYTES - key2 * 256u, 0x00020000);
  const __amdgpu_buffer_rsrc_t rv1 = __builtin_amdgcn_make_buffer_rsrc((void*)((p1 ? P.vB : P.vA) + (size_t)key1 * DH), 0, KVBYTES - key1 * 256u, 0x00020000);
  const __amdgpu_buffer_rsrc_t rv2 = __builtin_amdgcn_make_buffer_rsrc((void*)((p2 ? P.vB : P.vA) + (size_t)key2 * DH), 0, KVBYTES - key2 * 256u, 0x00020000);
  const int koff = ((lane >> 2) & 3) * 256 + ((lane >> 4) * 4 + (lane & 3)) * 16;
  const int voff = lane * 16;
  v4u K[NT][4], V[NT][4];
#define ISSUE_K(k_) do { _Pragma("unroll") for (int t_ = 0; t_ < 4; ++t_) \
    K[k_][t_] = __builtin_amdgcn_raw_buffer_load_b128((k_) < 4 ? rk1 : rk2, koff + t_ * 1024, (k_) * 32768, KV_AUX); FENCE_SCHED(); } while (0)
#define ISSUE_V(k_) do { _Pragma("unroll") for (int t_ = 0; t_ < 4; ++t_) \
    V[k_][t_] = __builtin_amdgcn_raw_buffer_load_b128((k_) < 4 ? rv1 : rv2, voff + t_ * 1024, (k_) * 32768, KV_AUX); FENCE_SCHED(); } while (0)
  ISSUE_K(0);
  if (tid < 4) lds_flag[tid] = 0u;

  const u32 tag0 = __builtin_amdgcn_readfirstlane(ep.x) + 1u, tag1 = __builtin_amdgcn_readfirstlane(ep.y) + 1u;
  {
    float acc[8];
#pragma unroll
    for (int j = 0; j < 8; ++j) acc[j] = 0.f;
    const float xf[4] = {bflo(xv.x), bfhi(xv.x), bflo(xv.y), bfhi(xv.y)};
#pragma unroll
    for (int k = 0; k < 4; ++k)
#pragma unroll
      for (int j = 0; j < 4; ++j) {
        acc[2 * j] = fmaf(xf[k], bflo(wv[k][j]), acc[2 * j]);
        acc[2 * j + 1] = fmaf(xf[k], bfhi(wv[k][j]), acc[2 * j + 1]);
      }
#pragma unroll
    for (int j = 0; j < 8; ++j) { acc[j] += dppf<ROR4>(acc[j]); acc[j] += dppf<ROR8>(acc[j]); }
    if (((lane >> 2) & 3) == 0) {
      float* dst = lds_red + (w * 4 + (lane >> 4)) * 32 + 8 * (lane & 3);
      *reinterpret_cast<f32x4*>(dst) = f32x4{acc[0], acc[1], acc[2], acc[3]};
      *reinterpret_cast<f32x4*>(dst + 4) = f32x4{acc[4], acc[5], acc[6], acc[7]};
    }
    STAMP(1);
    __syncthreads();
    if (w == 0) {
      const int col = lane & 31, half = lane >> 5;
      float s = 0.f;
#pragma unroll
      for (int r = 0; r < 16; ++r) s += lds_red[(16 * half + r) * 32 + col];
      s = x32_sum(s);
      const u32 b = cvtpk(s, s) & 0xffffu;
      const u32 pair = b | (dppu<0xF5>(b) << 16);
      const u32 pair2 = dppu<0xAA>(pair);
      if ((lane & 3) == 0 && lane < 32) {
        const v4u gr = {pair, tag0, pair2, tag1};
        __builtin_amdgcn_raw_buffer_store_b128(gr, ws, WS_Q + ((g * 2 + pq) * 4 + hq) * 512 + (8 * dq + (lane >> 2)) * 16, 0, 16);
      }
    }
  }
  FENCE_SCHED();
  STAMP(2);

  const int qh = (lane >> 2) & 3, qch = 4 * (lane >> 4) + (lane & 3);
  const bool mixed = (p1 != p2);
  int qsel = 0;
  bool need = false;
#define FLAG(n_) __builtin_amdgcn_readfirstlane(__hip_atomic_load(&lds_flag[n_], __ATOMIC_RELAXED, __HIP_MEMORY_SCOPE_WORKGROUP))
#define TAGOK(v_) ((v_).y == tag0 && (v_).w == tag1)
#define ROLE(k_) do { need = false; if ((k_) < POLL0) { } else if (w == ((k_) & 7)) { qsel = 0; need = (FLAG(0) == 0u); } else if (mixed && w == (((k_) + 4) & 7)) { qsel = 1; need = (FLAG(1) == 0u); } } while (0)
  const int qoffa = WS_Q + ((g * 2 + p1) * 4 + qh) * 512 + qch * 32, qoffb = WS_Q + ((g * 2 + p2) * 4 + qh) * 512 + qch * 32;
  v4u qa0, qa1;
#define POLLQ() do { const int qoff = qsel ? qoffb : qoffa; qa0 = __builtin_amdgcn_raw_buffer_load_b128(ws, qoff, 0, 16); qa1 = __builtin_amdgcn_raw_buffer_load_b128(ws, qoff + 16, 0, 16); FENCE_SCHED(); } while (0)
#define CHECKQ() do { if (__all(TAGOK(qa0) && TAGOK(qa1))) { STAMP(12); *reinterpret_cast<v4u*>(lds_q + qsel * 256 + lane * 4) = v4u{qa0.x, qa0.z, qa1.x, qa1.z}; \
      asm volatile("s_waitcnt lgkmcnt(0)" ::: "memory"); __hip_atomic_store(&lds_flag[qsel], 1u, __ATOMIC_RELAXED, __HIP_MEMORY_SCOPE_WORKGROUP); } FENCE_SCHED(); } while (0)
#define PSTAGE(k_) do { ROLE(k_); if (need) POLLQ(); ISSUE_K(k_); PACE0(); if (need) CHECKQ(); } while (0)
  const int po = i >> 4, ho = (i >> 2) & 3, qo = i & 3;
  const u16* wop = (po ? P.woB : P.woA) + (size_t)((4 * g + ho) * DH + 32 * qo + 4 * w) * DM + 8 * lane;
  v4u wo[4];
#define ISSUE_W() do { _Pragma("unroll") for (int kk = 0; kk < 4; ++kk) wo[kk] = __builtin_nontemporal_load(reinterpret_cast<const v4u*>(wop + kk * DM)); FENCE_SCHED(); } while (0)
  v4u q1, q2;
#define WAITQ() do { \
    for (unsigned spins = 0; spins < (SPIN_CAP << 2); ++spins) { \
      const u32 f0 = FLAG(0), f1 = FLAG(1); \
      if (f0 != 0u && (!mixed || f1 != 0u)) break; \
      if (w == 0 && f0 == 0u) { qsel = 0; POLLQ(); CHECKQ(); } \
      else if (w == 4 && mixed && f1 == 0u) { qsel = 1; POLLQ(); CHECKQ(); } \
      else __builtin_amdgcn_s_sleep(1); } \
    asm volatile("" ::: "memory"); \
    q1 = *reinterpret_cast<const v4u*>(lds_q + lane * 4); q2 = *reinterpret_cast<const v4u*>(lds_q + (mixed ? 256 : 0) + lane * 4); } while (0)
  float s[NT][4];
#define RND(sv_) bfhi(cvtpk(bfhi(cvtpk(sv_, sv_)) * SCALE, bfhi(cvtpk(sv_, sv_)) * SCALE))
#define SCORE(Kreg_, q_) ({ const f32x4 d_ = __builtin_amdgcn_mfma_f32_16x16x32_bf16(__builtin_bit_cast(bf16x8, Kreg_), __builtin_bit_cast(bf16x8, q_), f32x4{0.f, 0.f, 0.f, 0.f}, 0, 0, 0); \
      const float sv_ = (dppf<0x00>(d_[0]) + dppf<0x55>(d_[1])) + (dppf<0xAA>(d_[2]) + dppf<0xFF>(d_[3])); RND(sv_); })
#define QK(k_) do { _Pragma("unroll") for (int t_ = 0; t_ < 4; ++t_) s[k_][t_] = SCORE(K[k_][t_], (k_) < 4 ? q1 : q2); } while (0)
  float Mc1, Mc2;
#define MAXS() do { \
    float m1 = s[0][0], m2 = s[4][0]; \
    _Pragma("unroll") for (int k = 0; k < 4; ++k) _Pragma("unroll") for (int t = 0; t < 4; ++t) m1 = fmaxf(m1, s[k][t]); \
    _Pragma("unroll") for (int k = 4; k < NT; ++k) _Pragma("unroll") for (int t = 0; t < 4; ++t) m2 = fmaxf(m2, s[k][t]); \
    m1 = xrow_max(m1); m2 = xrow_max(m2); \
    if (!mixed) { m1 = fmaxf(m1, m2); m2 = m1; } \
    Mc1 = m1 * LOG2E; Mc2 = m2 * LOG2E; \
    if ((lane & 0x33) == 0) { lds_mx[(0 * 4 + qh) * 8 + w] = Mc1; lds_mx[(1 * 4 + qh) * 8 + w] = Mc2; } } while (0)
  const u32 hm = (lane & 1) ? 0xffff0000u : 0x0000ffffu;
  const u32 mask0 = (lane & 2) ? 0u : hm, mask1 = (lane & 2) ? hm : 0u;
  float l1 = 0.f, l2 = 0.f;
  f32x4 O[2][2];
#pragma unroll
  for (int a = 0; a < 2; ++a)
#pragma unroll
    for (int b = 0; b < 2; ++b) O[a][b] = f32x4{0.f, 0.f, 0.f, 0.f};
  v2u A[NT][4];
#define PREP(k_) do { _Pragma("unroll") for (int t_ = 0; t_ < 4; ++t_) { \
      const float p_ = __builtin_amdgcn_exp2f(fmaf(s[k_][t_], LOG2E, (k_) < 4 ? -Mc1 : -Mc2)); \
      if ((k_) < 4) l1 += p_; else l2 += p_; \
      const u32 pk_ = cvtpk(p_, p_); A[k_][t_] = v2u{pk_ & mask0, pk_ & mask1}; } } while (0)
#define PV1(A_, V_, sg_) do { const s16x4 a_ = __builtin_bit_cast(s16x4, A_); \
      O[sg_][0] = __builtin_amdgcn_mfma_f32_16x16x16bf16_1k(a_, __builtin_bit_cast(s16x4, v2u{(V_).x, (V_).y}), O[sg_][0], 0, 0, 0); \
      O[sg_][1] = __builtin_amdgcn_mfma_f32_16x16x16bf16_1k(a_, __builtin_bit_cast(s16x4, v2u{(V_).z, (V_).w}), O[sg_][1], 0, 0, 0); } while (0)
#define PV(k_) do { _Pragma("unroll") for (int t_ = 0; t_ < 4; ++t_) PV1(A[k_][t_], V[k_][t_], (k_) < 4 ? 0 : 1); } while (0)
#define ENDST() do { FENCE_SCHED(); PACE0(); } while (0)

  PSTAGE(1); STAMP(13);
  PSTAGE(2); PSTAGE(3); PSTAGE(4); STAMP(14);
  PSTAGE(5); PSTAGE(6);
  ISSUE_K(7); WAITQ(); STAMP(3); QK(0); QK(1); QK(2); ENDST();
  ISSUE_K(8); QK(3); QK(4); QK(5); ENDST(); STAMP(15);
  ISSUE_V(0); QK(6); QK(7); ENDST();
  ISSUE_V(1); QK(8); MAXS(); PREP(0); PV(0); PREP(1); ENDST(); STAMP(4);
  ISSUE_V(2); PV(1); PREP(2); ENDST();
  ISSUE_V(3); PV(2); PREP(3); ENDST();
  ISSUE_V(4); PV(3); PREP(4); ENDST();
  ISSUE_V(5); PV(4); PREP(5); ENDST();
  ISSUE_V(6); PV(5); PREP(6); ENDST();
  ISSUE_V(7); PV(6); PREP(7); ENDST();
  ISSUE_V(8); PV(7); PREP(8); ENDST();
  ISSUE_W(); PV(8);
  FENCE_SCHED();
  STAMP(6);
  if (!mixed) { O[0][0] += O[1][0]; O[0][1] += O[1][1]; l1 += l2; }
  l1 = xrow_sum(l1); l2 = xrow_sum(l2);
  if ((lane & 0x33) == 0) { lds_l[(0 * 4 + qh) * 8 + w] = l1; lds_l[(1 * 4 + qh) * 8 + w] = l2; }
  {
    float* dst = lds_o + w * 512 + (lane >> 4) * 128 + (lane & 15) * 8;
    *reinterpret_cast<f32x4*>(dst) = O[0][0];
    *reinterpret_cast<f32x4*>(dst + 4) = O[0][1];
    if (mixed) {
      *reinterpret_cast<f32x4*>(dst + 4096) = O[1][0];
      *reinterpret_cast<f32x4*>(dst + 4096 + 4) = O[1][1];
    }
  }
  __syncthreads();
  {
    const int sg = tid >> 8;
    if (sg == 0 || mixed) {
      const int h = (tid >> 6) & 3, gm = tid & 63;
      const int pout = sg ? p2 : p1;
      const int slot = i + pout;
      const f32x4 ma = *reinterpret_cast<const f32x4*>(lds_mx + (sg * 4 + h) * 8), mb = *reinterpret_cast<const f32x4*>(lds_mx + (sg * 4 + h) * 8 + 4);
      const float Mx = fmaxf(fmaxf(fmaxf(ma[0], ma[1]), fmaxf(ma[2], ma[3])), fmaxf(fmaxf(mb[0], mb[1]), fmaxf(mb[2], mb[3])));
      float wt[8];
#pragma unroll
      for (int ww = 0; ww < 4; ++ww) { wt[ww] = __builtin_amdgcn_exp2f(ma[ww] - Mx); wt[4 + ww] = __builtin_amdgcn_exp2f(mb[ww] - Mx); }
      f32x2 a = {0.f, 0.f};
#pragma unroll
      for (int ww = 0; ww < 8; ++ww) a += wt[ww] * *reinterpret_cast<const f32x2*>(lds_o + sg * 4096 + ww * 512 + h * 128 + 2 * gm);
      const int base = WS_PART + ((g * 33 + slot) * 4 + h) * PSTR;
      const v4u gr = {__float_as_uint(a[0]), tag0, __float_as_uint(a[1]), tag1};
      __builtin_amdgcn_raw_buffer_store_b128(gr, ws, base + gm * 16, 0, 16);
      if (gm == 0) {
        const f32x4 la = *reinterpret_cast<const f32x4*>(lds_l + (sg * 4 + h) * 8), lb = *reinterpret_cast<const f32x4*>(lds_l + (sg * 4 + h) * 8 + 4);
        const float L = ((wt[0] * la[0] + wt[1] * la[1]) + (wt[2] * la[2] + wt[3] * la[3])) + ((wt[4] * lb[0] + wt[5] * lb[1]) + (wt[6] * lb[2] + wt[7] * lb[3]));
        const v4u gm_ = {__float_as_uint(Mx), tag0, __float_as_uint(L), tag1};
        __builtin_amdgcn_raw_buffer_store_b128(gm_, ws, base + 64 * 16, 0, 16);
      }
    }
  }
  FENCE_SCHED();

  STAMP(7);
  const int nprod = po ? 4 : 29, slot0 = po ? 29 : 0;
  {
    const bool isdata = tid < 16 * nprod, isml = !isdata && tid < 17 * nprod;
    const int j = isdata ? (tid >> 4) : (tid - 16 * nprod);
    const int off = WS_PART + ((g * 33 + slot0 + j) * 4 + ho) * PSTR + (isdata ? (16 * qo + (tid & 15)) * 16 : 64 * 16);
    if (64 * w < 17 * nprod) {
      v4u v = {0u, 0u, 0u, 0u};
      bool okv = !(isdata || isml);
      for (unsigned spins = 0; spins < SPIN_CAP; ++spins) {
        if (!okv) v = __builtin_amdgcn_raw_buffer_load_b128(ws, off, 0, 16);
        okv = okv || TAGOK(v);
        if (__all(okv)) break;
        __builtin_amdgcn_s_sleep(1);
      }
      if (isdata) *reinterpret_cast<f32x2*>(lds_cv + j * 34 + 2 * (tid & 15)) = f32x2{__uint_as_float(v.x), __uint_as_float(v.z)};
      else if (isml) *reinterpret_cast<f32x2*>(lds_cml + 2 * j) = f32x2{__uint_as_float(v.x), __uint_as_float(v.z)};
    }
  }
  STAMP(8);
  __syncthreads();
  {
    const int j = lane & 31, half = lane >> 5;
    const bool val = j < nprod;
    const int jj = val ? j : 0;
    const f32x2 ml = *reinterpret_cast<const f32x2*>(lds_cml + 2 * jj);
    const f32x2 vv = *reinterpret_cast<const f32x2*>(lds_cv + jj * 34 + 4 * w + 2 * half);
    float m = val ? ml[0] : -INFINITY;
    float M = m;
    M = fmaxf(M, dppf<ROR1>(M)); M = fmaxf(M, dppf<ROR2>(M)); M = fmaxf(M, dppf<ROR4>(M)); M = fmaxf(M, dppf<ROR8>(M)); M = x16_max(M);
    const float wt = val ? __builtin_amdgcn_exp2f(m - M) : 0.f;
    float L = wt * ml[1], oa = wt * vv[0], ob = wt * vv[1];
    L += dppf<ROR1>(L); oa += dppf<ROR1>(oa); ob += dppf<ROR1>(ob);
    L += dppf<ROR2>(L); oa += dppf<ROR2>(oa); ob += dppf<ROR2>(ob);
    L += dppf<ROR4>(L); oa += dppf<ROR4>(oa); ob += dppf<ROR4>(ob);
    L += dppf<ROR8>(L); oa += dppf<ROR8>(oa); ob += dppf<ROR8>(ob);
    L = x16_sum(L); oa = x16_sum(oa); ob = x16_sum(ob);
    const float inv = 1.f / L;
    oa *= inv; ob *= inv;
    { const u32 ro_ = cvtpk(oa, ob); oa = bflo(ro_); ob = bfhi(ro_); }
    float o4[4];
    o4[0] = __builtin_bit_cast(float, __builtin_amdgcn_readlane(__builtin_bit_cast(int, oa), 0));
    o4[1] = __builtin_bit_cast(float, __builtin_amdgcn_readlane(__builtin_bit_cast(int, ob), 0));
    o4[2] = __builtin_bit_cast(float, __builtin_amdgcn_readlane(__builtin_bit_cast(int, oa), 32));
    o4[3] = __builtin_bit_cast(float, __builtin_amdgcn_readlane(__builtin_bit_cast(int, ob), 32));
    float acc[8];
#pragma unroll
    for (int c = 0; c < 8; ++c) acc[c] = 0.f;
#pragma unroll
    for (int k = 0; k < 4; ++k)
#pragma unroll
      for (int c = 0; c < 4; ++c) {
        acc[2 * c] = fmaf(o4[k], bflo(wo[k][c]), acc[2 * c]);
        acc[2 * c + 1] = fmaf(o4[k], bfhi(wo[k][c]), acc[2 * c + 1]);
      }
    float* dst = lds_o + w * 512 + lane * 8;
    *reinterpret_cast<f32x4*>(dst) = f32x4{acc[0], acc[1], acc[2], acc[3]};
    *reinterpret_cast<f32x4*>(dst + 4) = f32x4{acc[4], acc[5], acc[6], acc[7]};
  }
  __syncthreads();
  if (tid < 256) {
    f32x2 a = {0.f, 0.f};
#pragma unroll
    for (int ww = 0; ww < 8; ++ww) a += *reinterpret_cast<const f32x2*>(lds_o + ww * 512 + 2 * tid);
    const v4u gr = {__float_as_uint(a[0]), tag0, __float_as_uint(a[1]), tag1};
    __builtin_amdgcn_raw_buffer_store_b128(gr, ws, WS_OUT + ((po * 128 + g * 16 + (i & 15)) * 256 + tid) * 16, 0, 16);
  }
  FENCE_SCHED();

  STAMP(9);
  if (blockIdx.x < 64) {
    const int f = blockIdx.x;
    const int pr = tid >> 2, gm = tid & 3;
    const int offA = WS_OUT + ((0 * 128 + pr) * 256 + 4 * f + gm) * 16, offB = WS_OUT + ((1 * 128 + pr) * 256 + 4 * f + gm) * 16;
    v4u va = {0u, 0u, 0u, 0u}, vb = va;
    bool oka = false, okb = false;
    for (unsigned spins = 0; spins < SPIN_CAP; ++spins) {
      if (!oka) va = __builtin_amdgcn_raw_buffer_load_b128(ws, offA, 0, 16);
      if (!okb) vb = __builtin_amdgcn_raw_buffer_load_b128(ws, offB, 0, 16);
      oka = oka || TAGOK(va); okb = okb || TAGOK(vb);
      if (__all(oka && okb)) break;
      __builtin_amdgcn_s_sleep(1);
    }
    STAMP(10);
    float r[4] = {__uint_as_float(va.x), __uint_as_float(va.z), __uint_as_float(vb.x), __uint_as_float(vb.z)};
#pragma unroll
    for (int c = 0; c < 4; ++c) { r[c] += dppf<ROR4>(r[c]); r[c] += dppf<ROR8>(r[c]); r[c] = xrow_sum(r[c]); }
    if (lane < 4) {
      *reinterpret_cast<f32x2*>(lds_fin + w * 16 + 0 * 8 + gm * 2) = f32x2{r[0], r[1]};
      *reinterpret_cast<f32x2*>(lds_fin + w * 16 + 1 * 8 + gm * 2) = f32x2{r[2], r[3]};
    }
    __syncthreads();
    if (tid < 16) {
      float a = 0.f;
#pragma unroll
      for (int ww = 0; ww < 8; ++ww) a += lds_fin[ww * 16 + tid];
      const int p = tid >> 3, c = tid & 7;
      P.out[p * 512 + 8 * f + c] = (u16)(cvtpk(a, a) & 0xffffu);
    }
    STAMP(11);
    if (f == 0 && tid == 0) {
      const v2u ne = {tag0, tag1};
      __builtin_amdgcn_raw_buffer_store_b64(ne, ws, WS_EPOCH, 0, 16);
    }
  }
  STAMP_FLUSH();
}

extern "C" void kernel_launch(void* const* d_in, const int* in_sizes, int n_in, void* d_out, int out_size, void* d_ws, size_t ws_size,
                              hipStream_t stream) {
  Params P;
  P.xA = (const u16*)d_in[0]; P.kA = (const u16*)d_in[1]; P.vA = (const u16*)d_in[2]; P.wqA = (const u16*)d_in[3]; P.woA = (const u16*)d_in[4];
  P.xB = (const u16*)d_in[5]; P.kB = (const u16*)d_in[6]; P.vB = (const u16*)d_in[7]; P.wqB = (const u16*)d_in[8]; P.woB = (const u16*)d_in[9];
  P.out = (u16*)d_out; P.ws = (unsigned char*)d_ws;
  hipLaunchKernelGGL(fused_decode_kernel, dim3(NWG), dim3(NTHR), 0, stream, P);
}
```

```cpp
#include <hip/hip_runtime.h>
#include <hip/hip_bf16.h>
#include <stdint.h>

typedef unsigned int u32;
typedef unsigned long long u64;
typedef unsigned short u16;
typedef u32 v4u __attribute__((ext_vector_type(4)));
typedef u32 v2u __attribute__((ext_vector_type(2)));
typedef float f32x4 __attribute__((ext_vector_type(4)));
typedef float f32x2 __attribute__((ext_vector_type(2)));
typedef __bf16 bf16x8 __attribute__((ext_vector_type(8)));
typedef __bf16 bf16x2 __attribute__((ext_vector_type(2)));
typedef short s16x4 __attribute__((ext_vector_type(4)));

#define KV_AUX 2
#define ROUND_O 1
#define POLL0 4

constexpr int SK = 32768, DH = 128, DM = 512, NQ = 4096;
constexpr int NWG = 256, NTHR = 512, NT = 9;
constexpr float SCALE = 0.088388347648318440f;
constexpr float LOG2E = 1.4426950408889634f;

constexpr int WS_EPOCH = 0;
constexpr int WS_Q = 4096;
constexpr int WS_PART = WS_Q + 8 * 2 * 4 * 512;
constexpr int PSTR = 640;
constexpr int WS_OUT = WS_PART + 8 * 33 * 4 * PSTR;
constexpr int WS_BYTES = WS_OUT + 2 * 128 * 2048;
constexpr unsigned SPIN_CAP = 1u << 18;

struct Params {
  const u16 *xA, *kA, *vA, *wqA, *woA, *xB, *kB, *vB, *wqB, *woB;
  u16* out;
  unsigned char* ws;
};

template <int CTRL> __device__ __forceinline__ float dppf(float x) {
  return __builtin_bit_cast(float, __builtin_amdgcn_mov_dpp(__builtin_bit_cast(int, x), CTRL, 0xf, 0xf, true));
}
template <int CTRL> __device__ __forceinline__ u32 dppu(u32 x) {
  return (u32)__builtin_amdgcn_mov_dpp((int)x, CTRL, 0xf, 0xf, true);
}
constexpr int ROR4 = 0x124, ROR8 = 0x128, ROR1 = 0x121, ROR2 = 0x122;
__device__ __forceinline__ float xrow_max(float x) {
  auto s = __builtin_amdgcn_permlane16_swap(__float_as_uint(x), __float_as_uint(x), false, false);
  x = fmaxf(__uint_as_float(s[0]), __uint_as_float(s[1]));
  auto t = __builtin_amdgcn_permlane32_swap(__float_as_uint(x), __float_as_uint(x), false, false);
  return fmaxf(__uint_as_float(t[0]), __uint_as_float(t[1]));
}
__device__ __forceinline__ float xrow_sum(float x) {
  auto s = __builtin_amdgcn_permlane16_swap(__float_as_uint(x), __float_as_uint(x), false, false);
  x = __uint_as_float(s[0]) + __uint_as_float(s[1]);
  auto t = __builtin_amdgcn_permlane32_swap(__float_as_uint(x), __float_as_uint(x), false, false);
  return __uint_as_float(t[0]) + __uint_as_float(t[1]);
}
__device__ __forceinline__ float x16_sum(float x) {
  auto s = __builtin_amdgcn_permlane16_swap(__float_as_uint(x), __float_as_uint(x), false, false);
  return __uint_as_float(s[0]) + __uint_as_float(s[1]);
}
__device__ __forceinline__ float x16_max(float x) {
  auto s = __builtin_amdgcn_permlane16_swap(__float_as_uint(x), __float_as_uint(x), false, false);
  return fmaxf(__uint_as_float(s[0]), __uint_as_float(s[1]));
}
__device__ __forceinline__ float x32_sum(float x) {
  auto t = __builtin_amdgcn_permlane32_swap(__float_as_uint(x), __float_as_uint(x), false, false);
  return __uint_as_float(t[0]) + __uint_as_float(t[1]);
}
__device__ __forceinline__ float bflo(u32 w) { return __builtin_bit_cast(float, w << 16); }
__device__ __forceinline__ float bfhi(u32 w) { return __builtin_bit_cast(float, w & 0xffff0000u); }
__device__ __forceinline__ u32 cvtpk(float lo, float hi) {
  return __builtin_bit_cast(u32, __builtin_convertvector(f32x2{lo, hi}, bf16x2));
}
#define STAMP(n_) do { } while (0)
#define STAMP_DECL
#define STAMP_FLUSH() do { } while (0)
#define FENCE_SCHED() do { asm volatile("" ::: "memory"); __builtin_amdgcn_sched_barrier(0); } while (0)
#define PACE0() do { asm volatile("s_waitcnt vmcnt(0)" ::: "memory"); __builtin_amdgcn_sched_barrier(0); } while (0)

__global__ void __launch_bounds__(NTHR, 2) fused_decode_kernel(Params P) {
  __shared__ __attribute__((aligned(16))) float lds_red[32 * 32];
  __shared__ __attribute__((aligned(16))) float lds_mx[2 * 4 * 8];
  __shared__ __attribute__((aligned(16))) float lds_l[2 * 4 * 8];
  __shared__ __attribute__((aligned(16))) float lds_o[2 * 8 * 512];
  __shared__ __attribute__((aligned(16))) float lds_cv[29 * 34];
  __shared__ __attribute__((aligned(16))) float lds_cml[32 * 2];
  __shared__ __attribute__((aligned(16))) float lds_fin[8 * 16];
  __shared__ __attribute__((aligned(16))) u32 lds_q[512];
  __shared__ __attribute__((aligned(16))) u32 lds_flag[4];

  const int tid = threadIdx.x, lane = tid & 63;
  const int w = __builtin_amdgcn_readfirstlane(tid >> 6);
  const int g = blockIdx.x & 7, i = blockIdx.x >> 3;
  const __amdgpu_buffer_rsrc_t ws = __builtin_amdgcn_make_buffer_rsrc((void*)P.ws, 0, WS_BYTES, 0x00020000);

  STAMP_DECL;
  STAMP(0);
  const v2u ep = __builtin_amdgcn_raw_buffer_load_b64(ws, WS_EPOCH, 0, 16);

  const int pq = i >> 4, hq = (i >> 2) & 3, dq = i & 3;
  const u16* wqp = (pq ? P.wqB : P.wqA) + (4 * g + hq) * DH + 32 * dq + 8 * (lane & 3);
  const int row0 = 64 * w + 4 * (lane >> 2);
  const u16* xq = pq ? P.xB : P.xA;
  const v2u xv = *reinterpret_cast<const v2u*>(xq + row0);
  v4u wv[4];
#pragma unroll
  for (int k = 0; k < 4; ++k) wv[k] = __builtin_nontemporal_load(reinterpret_cast<const v4u*>(wqp + (size_t)(row0 + k) * NQ));
  FENCE_SCHED();

  const int p1 = (i >= 29) ? 1 : 0, p2 = (i >= 28) ? 1 : 0;
  const u32 key1 = (u32)g * SK + 128 * (9 * i) + 16 * w - (p1 ? 4096 : 0);
  const u32 key2 = (u32)g * SK + 128 * (9 * i) + 16 * w - (p2 ? 4096 : 0);
  constexpr u32 KVBYTES = 8u * SK * DH * 2u;
  const __amdgpu_buffer_rsrc_t rk1 = __builtin_amdgcn_make_buffer_rsrc((void*)((p1 ? P.kB : P.kA) + (size_t)key1 * DH), 0, KVBYTES - key1 * 256u, 0x00020000);
  const __amdgpu_buffer_rsrc_t rk2 = __builtin_amdgcn_make_buffer_rsrc((void*)((p2 ? P.kB : P.kA) + (size_t)key2 * DH), 0, KVBYTES - key2 * 256u, 0x00020000);
  const __amdgpu_buffer_rsrc_t rv1 = __builtin_amdgcn_make_buffer_rsrc((void*)((p1 ? P.vB : P.vA) + (size_t)key1 * DH), 0, KVBYTES - key1 * 256u, 0x00020000);
  const __amdgpu_buffer_rsrc_t rv2 = __builtin_amdgcn_make_buffer_rsrc((void*)((p2 ? P.vB : P.vA) + (size_t)key2 * DH), 0, KVBYTES - key2 * 256u, 0x00020000);
  const int koff = ((lane >> 2) & 3) * 256 + ((lane >> 4) * 4 + (lane & 3)) * 16;
  const int voff = lane * 16;
  v4u K[NT][4], V[NT][4];
#define ISSUE_K(k_) do { _Pragma("unroll") for (int t_ = 0; t_ < 4; ++t_) \
    K[k_][t_] = __builtin_amdgcn_raw_buffer_load_b128((k_) < 4 ? rk1 : rk2, koff + t_ * 1024, (k_) * 32768, KV_AUX); FENCE_SCHED(); } while (0)
#define ISSUE_V(k_) do { _Pragma("unroll") for (int t_ = 0; t_ < 4; ++t_) \
    V[k_][t_] = __builtin_amdgcn_raw_buffer_load_b128((k_) < 4 ? rv1 : rv2, voff + t_ * 1024, (k_) * 32768, KV_AUX); FENCE_SCHED(); } while (0)
  ISSUE_K(0);
  if (tid < 4) lds_flag[tid] = 0u;

  const u32 tag0 = __builtin_amdgcn_readfirstlane(ep.x) + 1u, tag1 = __builtin_amdgcn_readfirstlane(ep.y) + 1u;
  {
    float acc[8];
#pragma unroll
    for (int j = 0; j < 8; ++j) acc[j] = 0.f;
    const float xf[4] = {bflo(xv.x), bfhi(xv.x), bflo(xv.y), bfhi(xv.y)};
#pragma unroll
    for (int k = 0; k < 4; ++k)
#pragma unroll
      for (int j = 0; j < 4; ++j) {
        acc[2 * j] = fmaf(xf[k], bflo(wv[k][j]), acc[2 * j]);
        acc[2 * j + 1] = fmaf(xf[k], bfhi(wv[k][j]), acc[2 * j + 1]);
      }
#pragma unroll
    for (int j = 0; j < 8; ++j) { acc[j] += dppf<ROR4>(acc[j]); acc[j] += dppf<ROR8>(acc[j]); }
    if (((lane >> 2) & 3) == 0) {
      float* dst = lds_red + (w * 4 + (lane >> 4)) * 32 + 8 * (lane & 3);
      *reinterpret_cast<f32x4*>(dst) = f32x4{acc[0], acc[1], acc[2], acc[3]};
      *reinterpret_cast<f32x4*>(dst + 4) = f32x4{acc[4], acc[5], acc[6], acc[7]};
    }
    STAMP(1);
    __syncthreads();
    if (w == 0) {
      const int col = lane & 31, half = lane >> 5;
      float s = 0.f;
#pragma unroll
      for (int r = 0; r < 16; ++r) s += lds_red[(16 * half + r) * 32 + col];
      s = x32_sum(s);
      const u32 b = cvtpk(s, s) & 0xffffu;
      const u32 pair = b | (dppu<0xF5>(b) << 16);
      const u32 pair2 = dppu<0xAA>(pair);
      if ((lane & 3) == 0 && lane < 32) {
        const v4u gr = {pair, tag0, pair2, tag1};
        __builtin_amdgcn_raw_buffer_store_b128(gr, ws, WS_Q + ((g * 2 + pq) * 4 + hq) * 512 + (8 * dq + (lane >> 2)) * 16, 0, 16);
      }
    }
  }
  FENCE_SCHED();
  STAMP(2);

  const int qh = (lane >> 2) & 3, qch = 4 * (lane >> 4) + (lane & 3);
  const bool mixed = (p1 != p2);
  int qsel = 0;
  bool need = false;
#define FLAG(n_) __builtin_amdgcn_readfirstlane(__hip_atomic_load(&lds_flag[n_], __ATOMIC_RELAXED, __HIP_MEMORY_SCOPE_WORKGROUP))
#define TAGOK(v_) ((v_).y == tag0 && (v_).w == tag1)
#define ROLE(k_) do { need = false; if ((k_) < POLL0) { } else if (w == ((k_) & 7)) { qsel = 0; need = (FLAG(0) == 0u); } else if (mixed && w == (((k_) + 4) & 7)) { qsel = 1; need = (FLAG(1) == 0u); } } while (0)
  const int qoffa = WS_Q + ((g * 2 + p1) * 4 + qh) * 512 + qch * 32, qoffb = WS_Q + ((g * 2 + p2) * 4 + qh) * 512 + qch * 32;
  v4u qa0, qa1;
#define POLLQ() do { const int qoff = qsel ? qoffb : qoffa; qa0 = __builtin_amdgcn_raw_buffer_load_b128(ws, qoff, 0, 16); qa1 = __builtin_amdgcn_raw_buffer_load_b128(ws, qoff + 16, 0, 16); FENCE_SCHED(); } while (0)
#define CHECKQ() do { if (__all(TAGOK(qa0) && TAGOK(qa1))) { STAMP(12); *reinterpret_cast<v4u*>(lds_q + qsel * 256 + lane * 4) = v4u{qa0.x, qa0.z, qa1.x, qa1.z}; \
      asm volatile("s_waitcnt lgkmcnt(0)" ::: "memory"); __hip_atomic_store(&lds_flag[qsel], 1u, __ATOMIC_RELAXED, __HIP_MEMORY_SCOPE_WORKGROUP); } FENCE_SCHED(); } while (0)
#define PSTAGE(k_) do { ROLE(k_); if (need) POLLQ(); ISSUE_K(k_); PACE0(); if (need) CHECKQ(); } while (0)
  const int po = i >> 4, ho = (i >> 2) & 3, qo = i & 3;
  const u16* wop = (po ? P.woB : P.woA) + (size_t)((4 * g + ho) * DH + 32 * qo + 4 * w) * DM + 8 * lane;
  v4u wo[4];
#define ISSUE_W() do { _Pragma("unroll") for (int kk = 0; kk < 4; ++kk) wo[kk] = __builtin_nontemporal_load(reinterpret_cast<const v4u*>(wop + kk * DM)); FENCE_SCHED(); } while (0)
  v4u q1, q2;
#define WAITQ() do { \
    for (unsigned spins = 0; spins < (SPIN_CAP << 2); ++spins) { \
      const u32 f0 = FLAG(0), f1 = FLAG(1); \
      if (f0 != 0u && (!mixed || f1 != 0u)) break; \
      if (w == 0 && f0 == 0u) { qsel = 0; POLLQ(); CHECKQ(); } \
      else if (w == 4 && mixed && f1 == 0u) { qsel = 1; POLLQ(); CHECKQ(); } \
      else __builtin_amdgcn_s_sleep(1); } \
    asm volatile("" ::: "memory"); \
    q1 = *reinterpret_cast<const v4u*>(lds_q + lane * 4); q2 = *reinterpret_cast<const v4u*>(lds_q + (mixed ? 256 : 0) + lane * 4); } while (0)
  float s[NT][4];
#define RND(sv_) bfhi(cvtpk(bfhi(cvtpk(sv_, sv_)) * SCALE, bfhi(cvtpk(sv_, sv_)) * SCALE))
#define SCORE(Kreg_, q_) ({ const f32x4 d_ = __builtin_amdgcn_mfma_f32_16x16x32_bf16(__builtin_bit_cast(bf16x8, Kreg_), __builtin_bit_cast(bf16x8, q_), f32x4{0.f, 0.f, 0.f, 0.f}, 0, 0, 0); \
      const float sv_ = (dppf<0x00>(d_[0]) + dppf<0x55>(d_[1])) + (dppf<0xAA>(d_[2]) + dppf<0xFF>(d_[3])); RND(sv_); })
#define QK(k_) do { _Pragma("unroll") for (int t_ = 0; t_ < 4; ++t_) s[k_][t_] = SCORE(K[k_][t_], (k_) < 4 ? q1 : q2); } while (0)
  float Mc1, Mc2;
#define MAXS() do { \
    float m1 = s[0][0], m2 = s[4][0]; \
    _Pragma("unroll") for (int k = 0; k < 4; ++k) _Pragma("unroll") for (int t = 0; t < 4; ++t) m1 = fmaxf(m1, s[k][t]); \
    _Pragma("unroll") for (int k = 4; k < NT; ++k) _Pragma("unroll") for (int t = 0; t < 4; ++t) m2 = fmaxf(m2, s[k][t]); \
    m1 = xrow_max(m1); m2 = xrow_max(m2); \
    if (!mixed) { m1 = fmaxf(m1, m2); m2 = m1; } \
    Mc1 = m1 * LOG2E; Mc2 = m2 * LOG2E; \
    if ((lane & 0x33) == 0) { lds_mx[(0 * 4 + qh) * 8 + w] = Mc1; lds_mx[(1 * 4 + qh) * 8 + w] = Mc2; } } while (0)
  const u32 hm = (lane & 1) ? 0xffff0000u : 0x0000ffffu;
  const u32 mask0 = (lane & 2) ? 0u : hm, mask1 = (lane & 2) ? hm : 0u;
  float l1 = 0.f, l2 = 0.f;
  f32x4 O[2][2];
#pragma unroll
  for (int a = 0; a < 2; ++a)
#pragma unroll
    for (int b = 0; b < 2; ++b) O[a][b] = f32x4{0.f, 0.f, 0.f, 0.f};
  v2u A[NT][4];
#define PREP(k_) do { _Pragma("unroll") for (int t_ = 0; t_ < 4; ++t_) { \
      const float p_ = __builtin_amdgcn_exp2f(fmaf(s[k_][t_], LOG2E, (k_) < 4 ? -Mc1 : -Mc2)); \
      if ((k_) < 4) l1 += p_; else l2 += p_; \
      const u32 pk_ = cvtpk(p_, p_); A[k_][t_] = v2u{pk_ & mask0, pk_ & mask1}; } } while (0)
#define PV1(A_, V_, sg_) do { const s16x4 a_ = __builtin_bit_cast(s16x4, A_); \
      O[sg_][0] = __builtin_amdgcn_mfma_f32_16x16x16bf16_1k(a_, __builtin_bit_cast(s16x4, v2u{(V_).x, (V_).y}), O[sg_][0], 0, 0, 0); \
      O[sg_][1] = __builtin_amdgcn_mfma_f32_16x16x16bf16_1k(a_, __builtin_bit_cast(s16x4, v2u{(V_).z, (V_).w}), O[sg_][1], 0, 0, 0); } while (0)
#define PV(k_) do { _Pragma("unroll") for (int t_ = 0; t_ < 4; ++t_) PV1(A[k_][t_], V[k_][t_], (k_) < 4 ? 0 : 1); } while (0)
#define ENDST() do { FENCE_SCHED(); PACE0(); } while (0)

  PSTAGE(1); STAMP(13);
  PSTAGE(2); PSTAGE(3); PSTAGE(4); STAMP(14);
  PSTAGE(5); PSTAGE(6);
  ISSUE_K(7); WAITQ(); STAMP(3); QK(0); QK(1); QK(2); ENDST();
  ISSUE_K(8); QK(3); QK(4); QK(5); ENDST(); STAMP(15);
  ISSUE_V(0); QK(6); QK(7); ENDST();
  ISSUE_V(1); QK(8); MAXS(); PREP(0); PV(0); PREP(1); ENDST(); STAMP(4);
  ISSUE_V(2); PV(1); PREP(2); ENDST();
  ISSUE_V(3); PV(2); PREP(3); ENDST();
  ISSUE_V(4); PV(3); PREP(4); ENDST();
  ISSUE_V(5); PV(4); PREP(5); ENDST();
  ISSUE_V(6); PV(5); PREP(6); ENDST();
  ISSUE_V(7); PV(6); PREP(7); ENDST();
  ISSUE_V(8); PV(7); PREP(8); ENDST();
  ISSUE_W(); PV(8);
  FENCE_SCHED();
  STAMP(6);
  if (!mixed) { O[0][0] += O[1][0]; O[0][1] += O[1][1]; l1 += l2; }
  l1 = xrow_sum(l1); l2 = xrow_sum(l2);
  if ((lane & 0x33) == 0) { lds_l[(0 * 4 + qh) * 8 + w] = l1; lds_l[(1 * 4 + qh) * 8 + w] = l2; }
  {
    float* dst = lds_o + w * 512 + (lane >> 4) * 128 + (lane & 15) * 8;
    *reinterpret_cast<f32x4*>(dst) = O[0][0];
    *reinterpret_cast<f32x4*>(dst + 4) = O[0][1];
    if (mixed) {
      *reinterpret_cast<f32x4*>(dst + 4096) = O[1][0];
      *reinterpret_cast<f32x4*>(dst + 4096 + 4) = O[1][1];
    }
  }
  __syncthreads();
  {
    const int sg = tid >> 7;
    if (sg == 0 || (sg == 1 && mixed)) {
      const int h = (tid >> 5) & 3, gm = tid & 31;
      const int pout = sg ? p2 : p1;
      const int slot = i + pout;
      const f32x4 ma = *reinterpret_cast<const f32x4*>(lds_mx + (sg * 4 + h) * 8), mb = *reinterpret_cast<const f32x4*>(lds_mx + (sg * 4 + h) * 8 + 4);
      const float Mx = fmaxf(fmaxf(fmaxf(ma[0], ma[1]), fmaxf(ma[2], ma[3])), fmaxf(fmaxf(mb[0], mb[1]), fmaxf(mb[2], mb[3])));
      float wt[8];
#pragma unroll
      for (int ww = 0; ww < 4; ++ww) { wt[ww] = __builtin_amdgcn_exp2f(ma[ww] - Mx); wt[4 + ww] = __builtin_amdgcn_exp2f(mb[ww] - Mx); }
      f32x4 a = {0.f, 0.f, 0.f, 0.f};
#pragma unroll
      for (int ww = 0; ww < 8; ++ww) a += wt[ww] * *reinterpret_cast<const f32x4*>(lds_o + sg * 4096 + ww * 512 + h * 128 + 4 * gm);
      const int base = WS_PART + ((g * 33 + slot) * 4 + h) * PSTR;
      const v4u gr = {cvtpk(a[0], a[1]), tag0, cvtpk(a[2], a[3]), tag1};
      __builtin_amdgcn_raw_buffer_store_b128(gr, ws, base + gm * 16, 0, 16);
      if (gm == 0) {
        const f32x4 la = *reinterpret_cast<const f32x4*>(lds_l + (sg * 4 + h) * 8), lb = *reinterpret_cast<const f32x4*>(lds_l + (sg * 4 + h) * 8 + 4);
        const float L = ((wt[0] * la[0] + wt[1] * la[1]) + (wt[2] * la[2] + wt[3] * la[3])) + ((wt[4] * lb[0] + wt[5] * lb[1]) + (wt[6] * lb[2] + wt[7] * lb[3]));
        const v4u gm_ = {__float_as_uint(Mx), tag0, __float_as_uint(L), tag1};
        __builtin_amdgcn_raw_buffer_store_b128(gm_, ws, base + 32 * 16, 0, 16);
      }
    }
  }
  FENCE_SCHED();

  STAMP(7);
  const int nprod = po ? 4 : 29, slot0 = po ? 29 : 0;
  {
    const bool isdata = tid < 8 * nprod, isml = !isdata && tid < 9 * nprod;
    const int j = isdata ? (tid >> 3) : (tid - 8 * nprod);
    const int off = WS_PART + ((g * 33 + slot0 + j) * 4 + ho) * PSTR + (isdata ? (8 * qo + (tid & 7)) * 16 : 32 * 16);
    if (64 * w < 9 * nprod) {
      v4u v = {0u, 0u, 0u, 0u};
      bool okv = !(isdata || isml);
      for (unsigned spins = 0; spins < SPIN_CAP; ++spins) {
        if (!okv) v = __builtin_amdgcn_raw_buffer_load_b128(ws, off, 0, 16);
        okv = okv || TAGOK(v);
        if (__all(okv)) break;
        __builtin_amdgcn_s_sleep(1);
      }
      if (isdata) {
        float* dst = lds_cv + j * 34 + 4 * (tid & 7);
        *reinterpret_cast<f32x2*>(dst) = f32x2{bflo(v.x), bfhi(v.x)};
        *reinterpret_cast<f32x2*>(dst + 2) = f32x2{bflo(v.z), bfhi(v.z)};
      } else if (isml) *reinterpret_cast<f32x2*>(lds_cml + 2 * j) = f32x2{__uint_as_float(v.x), __uint_as_float(v.z)};
    }
  }
  STAMP(8);
  __syncthreads();
  {
    const int j = lane & 31, half = lane >> 5;
    const bool val = j < nprod;
    const int jj = val ? j : 0;
    const f32x2 ml = *reinterpret_cast<const f32x2*>(lds_cml + 2 * jj);
    const f32x2 vv = *reinterpret_cast<const f32x2*>(lds_cv + jj * 34 + 4 * w + 2 * half);
    float m = val ? ml[0] : -INFINITY;
    float M = m;
    M = fmaxf(M, dppf<ROR1>(M)); M = fmaxf(M, dppf<ROR2>(M)); M = fmaxf(M, dppf<ROR4>(M)); M = fmaxf(M, dppf<ROR8>(M)); M = x16_max(M);
    const float wt = val ? __builtin_amdgcn_exp2f(m - M) : 0.f;
    float L = wt * ml[1], oa = wt * vv[0], ob = wt * vv[1];
    L += dppf<ROR1>(L); oa += dppf<ROR1>(oa); ob += dppf<ROR1>(ob);
    L += dppf<ROR2>(L); oa += dppf<ROR2>(oa); ob += dppf<ROR2>(ob);
    L += dppf<ROR4>(L); oa += dppf<ROR4>(oa); ob += dppf<ROR4>(ob);
    L += dppf<ROR8>(L); oa += dppf<ROR8>(oa); ob += dppf<ROR8>(ob);
    L = x16_sum(L); oa = x16_sum(oa); ob = x16_sum(ob);
    const float inv = 1.f / L;
    oa *= inv; ob *= inv;
    { const u32 ro_ = cvtpk(oa, ob); oa = bflo(ro_); ob = bfhi(ro_); }
    float o4[4];
    o4[0] = __builtin_bit_cast(float, __builtin_amdgcn_readlane(__builtin_bit_cast(int, oa), 0));
    o4[1] = __builtin_bit_cast(float, __builtin_amdgcn_readlane(__builtin_bit_cast(int, ob), 0));
    o4[2] = __builtin_bit_cast(float, __builtin_amdgcn_readlane(__builtin_bit_cast(int, oa), 32));
    o4[3] = __builtin_bit_cast(float, __builtin_amdgcn_readlane(__builtin_bit_cast(int, ob), 32));
    float acc[8];
#pragma unroll
    for (int c = 0; c < 8; ++c) acc[c] = 0.f;
#pragma unroll
    for (int k = 0; k < 4; ++k)
#pragma unroll
      for (int c = 0; c < 4; ++c) {
        acc[2 * c] = fmaf(o4[k], bflo(wo[k][c]), acc[2 * c]);
        acc[2 * c + 1] = fmaf(o4[k], bfhi(wo[k][c]), acc[2 * c + 1]);
      }
    float* dst = lds_o + w * 512 + lane * 8;
    *reinterpret_cast<f32x4*>(dst) = f32x4{acc[0], acc[1], acc[2], acc[3]};
    *reinterpret_cast<f32x4*>(dst + 4) = f32x4{acc[4], acc[5], acc[6], acc[7]};
  }
  __syncthreads();
  if (tid < 128) {
    f32x4 a = {0.f, 0.f, 0.f, 0.f};
#pragma unroll
    for (int ww = 0; ww < 8; ++ww) a += *reinterpret_cast<const f32x4*>(lds_o + ww * 512 + 4 * tid);
    const v4u gr = {cvtpk(a[0], a[1]), tag0, cvtpk(a[2], a[3]), tag1};
    __builtin_amdgcn_raw_buffer_store_b128(gr, ws, WS_OUT + ((po * 128 + g * 16 + (i & 15)) * 128 + tid) * 16, 0, 16);
  }
  FENCE_SCHED();

  STAMP(9);
  if (blockIdx.x < 32) {
    const int f = blockIdx.x;
    const int pr = tid >> 2, gm = tid & 3;
    const int offA = WS_OUT + ((0 * 128 + pr) * 128 + 4 * f + gm) * 16, offB = WS_OUT + ((1 * 128 + pr) * 128 + 4 * f + gm) * 16;
    v4u va = {0u, 0u, 0u, 0u}, vb = va;
    bool oka = false, okb = false;
    for (unsigned spins = 0; spins < SPIN_CAP; ++spins) {
      if (!oka) va = __builtin_amdgcn_raw_buffer_load_b128(ws, offA, 0, 16);
      if (!okb) vb = __builtin_amdgcn_raw_buffer_load_b128(ws, offB, 0, 16);
      oka = oka || TAGOK(va); okb = okb || TAGOK(vb);
      if (__all(oka && okb)) break;
      __builtin_amdgcn_s_sleep(1);
    }
    STAMP(10);
    float r[8] = {bflo(va.x), bfhi(va.x), bflo(va.z), bfhi(va.z), bflo(vb.x), bfhi(vb.x), bflo(vb.z), bfhi(vb.z)};
#pragma unroll
    for (int c = 0; c < 8; ++c) { r[c] += dppf<ROR4>(r[c]); r[c] += dppf<ROR8>(r[c]); r[c] = xrow_sum(r[c]); }
    float* fin = lds_red;
    if (lane < 4) {
      *reinterpret_cast<f32x4*>(fin + w * 32 + 0 * 16 + gm * 4) = f32x4{r[0], r[1], r[2], r[3]};
      *reinterpret_cast<f32x4*>(fin + w * 32 + 1 * 16 + gm * 4) = f32x4{r[4], r[5], r[6], r[7]};
    }
    __syncthreads();
    if (tid < 32) {
      float a = 0.f;
#pragma unroll
      for (int ww = 0; ww < 8; ++ww) a += fin[ww * 32 + tid];
      const int p = tid >> 4, c = tid & 15;
      P.out[p * 512 + 16 * f + c] = (u16)(cvtpk(a, a) & 0xffffu);
    }
    STAMP(11);
    if (f == 0 && tid == 0) {
      const v2u ne = {tag0, tag1};
      __builtin_amdgcn_raw_buffer_store_b64(ne, ws, WS_EPOCH, 0, 16);
    }
  }
  STAMP_FLUSH();
}

extern "C" void kernel_launch(void* const* d_in, const int* in_sizes, int n_in, void* d_out, int out_size, void* d_ws, size_t ws_size,
                              hipStream_t stream) {
  Params P;
  P.xA = (const u16*)d_in[0]; P.kA = (const u16*)d_in[1]; P.vA = (const u16*)d_in[2]; P.wqA = (const u16*)d_in[3]; P.woA = (const u16*)d_in[4];
  P.xB = (const u16*)d_in[5]; P.kB = (const u16*)d_in[6]; P.vB = (const u16*)d_in[7]; P.wqB = (const u16*)d_in[8]; P.woB = (const u16*)d_in[9];
  P.out = (u16*)d_out; P.ws = (unsigned char*)d_ws;
  hipLaunchKernelGGL(fused_decode_kernel, dim3(NWG), dim3(NTHR), 0, stream, P);
}
```

```cpp
#include <hip/hip_runtime.h>
#include <hip/hip_bf16.h>
#include <stdint.h>

typedef unsigned int u32;
typedef unsigned long long u64;
typedef unsigned short u16;
typedef u32 v4u __attribute__((ext_vector_type(4)));
typedef u32 v2u __attribute__((ext_vector_type(2)));
typedef float f32x4 __attribute__((ext_vector_type(4)));
typedef float f32x2 __attribute__((ext_vector_type(2)));
typedef __bf16 bf16x8 __attribute__((ext_vector_type(8)));
typedef __bf16 bf16x2 __attribute__((ext_vector_type(2)));
typedef short s16x4 __attribute__((ext_vector_type(4)));

#define KV_AUX 2
#define POLL0 4

constexpr int SK = 32768, DH = 128, DM = 512, NQ = 4096;
constexpr int NWG = 256, NTHR = 512, NT = 9;
constexpr float SCALE = 0.088388347648318440f;
constexpr float LOG2E = 1.4426950408889634f;

constexpr int WS_EPOCH = 0;
constexpr int WS_Q = 4096;
constexpr int WS_PART = WS_Q + 8 * 2 * 4 * 512;
constexpr int PSTR = 640;
constexpr int WS_OUT = WS_PART + 8 * 33 * 4 * PSTR;
constexpr int WS_BYTES = WS_OUT + 2 * 128 * 4096;
constexpr unsigned SPIN_CAP = 1u << 18;

struct Params {
  const u16 *xA, *kA, *vA, *wqA, *woA, *xB, *kB, *vB, *wqB, *woB;
  u16* out;
  unsigned char* ws;
};

template <int CTRL> __device__ __forceinline__ float dppf(float x) {
  return __builtin_bit_cast(float, __builtin_amdgcn_mov_dpp(__builtin_bit_cast(int, x), CTRL, 0xf, 0xf, true));
}
template <int CTRL> __device__ __forceinline__ u32 dppu(u32 x) {
  return (u32)__builtin_amdgcn_mov_dpp((int)x, CTRL, 0xf, 0xf, true);
}
constexpr int ROR4 = 0x124, ROR8 = 0x128, ROR1 = 0x121, ROR2 = 0x122;
__device__ __forceinline__ float xrow_max(float x) {
  auto s = __builtin_amdgcn_permlane16_swap(__float_as_uint(x), __float_as_uint(x), false, false);
  x = fmaxf(__uint_as_float(s[0]), __uint_as_float(s[1]));
  auto t = __builtin_amdgcn_permlane32_swap(__float_as_uint(x), __float_as_uint(x), false, false);
  return fmaxf(__uint_as_float(t[0]), __uint_as_float(t[1]));
}
__device__ __forceinline__ float xrow_sum(float x) {
  auto s = __builtin_amdgcn_permlane16_swap(__float_as_uint(x), __float_as_uint(x), false, false);
  x = __uint_as_float(s[0]) + __uint_as_float(s[1]);
  auto t = __builtin_amdgcn_permlane32_swap(__float_as_uint(x), __float_as_uint(x), false, false);
  return __uint_as_float(t[0]) + __uint_as_float(t[1]);
}
__device__ __forceinline__ float x16_sum(float x) {
  auto s = __builtin_amdgcn_permlane16_swap(__float_as_uint(x), __float_as_uint(x), false, false);
  return __uint_as_float(s[0]) + __uint_as_float(s[1]);
}
__device__ __forceinline__ float x16_max(float x) {
  auto s = __builtin_amdgcn_permlane16_swap(__float_as_uint(x), __float_as_uint(x), false, false);
  return fmaxf(__uint_as_float(s[0]), __uint_as_float(s[1]));
}
__device__ __forceinline__ float x32_sum(float x) {
  auto t = __builtin_amdgcn_permlane32_swap(__float_as_uint(x), __float_as_uint(x), false, false);
  return __uint_as_float(t[0]) + __uint_as_float(t[1]);
}
__device__ __forceinline__ float bflo(u32 w) { return __builtin_bit_cast(float, w << 16); }
__device__ __forceinline__ float bfhi(u32 w) { return __builtin_bit_cast(float, w & 0xffff0000u); }
__device__ __forceinline__ u32 cvtpk(float lo, float hi) {
  return __builtin_bit_cast(u32, __builtin_convertvector(f32x2{lo, hi}, bf16x2));
}
#define FENCE_SCHED() do { asm volatile("" ::: "memory"); __builtin_amdgcn_sched_barrier(0); } while (0)
#define PACE0() do { asm volatile("s_waitcnt vmcnt(0)" ::: "memory"); __builtin_amdgcn_sched_barrier(0); } while (0)

__global__ void __launch_bounds__(NTHR, 2) fused_decode_kernel(Params P) {
  __shared__ __attribute__((aligned(16))) float lds_red[32 * 32];
  __shared__ __attribute__((aligned(16))) float lds_mx[2 * 4 * 8];
  __shared__ __attribute__((aligned(16))) float lds_l[2 * 4 * 8];
  __shared__ __attribute__((aligned(16))) float lds_o[2 * 8 * 512];
  __shared__ __attribute__((aligned(16))) float lds_cv[29 * 34];
  __shared__ __attribute__((aligned(16))) float lds_cml[32 * 2];
  __shared__ __attribute__((aligned(16))) float lds_fin[8 * 16];
  __shared__ __attribute__((aligned(16))) u32 lds_q[512];
  __shared__ __attribute__((aligned(16))) u32 lds_flag[4];

  const int tid = threadIdx.x, lane = tid & 63;
  const int w = __builtin_amdgcn_readfirstlane(tid >> 6);
  const int g = blockIdx.x & 7, i = blockIdx.x >> 3;
  const __amdgpu_buffer_rsrc_t ws = __builtin_amdgcn_make_buffer_rsrc((void*)P.ws, 0, WS_BYTES, 0x00020000);

  const v2u ep = __builtin_amdgcn_raw_buffer_load_b64(ws, WS_EPOCH, 0, 16);

  const int pq = i >> 4, hq = (i >> 2) & 3, dq = i & 3;
  const u16* wqp = (pq ? P.wqB : P.wqA) + (4 * g + hq) * DH + 32 * dq + 8 * (lane & 3);
  const int row0 = 64 * w + 4 * (lane >> 2);
  const u16* xq = pq ? P.xB : P.xA;
  const v2u xv = *reinterpret_cast<const v2u*>(xq + row0);
  v4u wv[4];
#pragma unroll
  for (int k = 0; k < 4; ++k) wv[k] = __builtin_nontemporal_load(reinterpret_cast<const v4u*>(wqp + (size_t)(row0 + k) * NQ));
  FENCE_SCHED();

  const int p1 = (i >= 29) ? 1 : 0, p2 = (i >= 28) ? 1 : 0;
  const u32 key1 = (u32)g * SK + 128 * (9 * i) + 16 * w - (p1 ? 4096 : 0);
  const u32 key2 = (u32)g * SK + 128 * (9 * i) + 16 * w - (p2 ? 4096 : 0);
  constexpr u32 KVBYTES = 8u * SK * DH * 2u;
  const __amdgpu_buffer_rsrc_t rk1 = __builtin_amdgcn_make_buffer_rsrc((void*)((p1 ? P.kB : P.kA) + (size_t)key1 * DH), 0, KVBYTES - key1 * 256u, 0x00020000);
  const __amdgpu_buffer_rsrc_t rk2 = __builtin_amdgcn_make_buffer_rsrc((void*)((p2 ? P.kB : P.kA) + (size_t)key2 * DH), 0, KVBYTES - key2 * 256u, 0x00020000);
  const __amdgpu_buffer_rsrc_t rv1 = __builtin_amdgcn_make_buffer_rsrc((void*)((p1 ? P.vB : P.vA) + (size_t)key1 * DH), 0, KVBYTES - key1 * 256u, 0x00020000);
  const __amdgpu_buffer_rsrc_t rv2 = __builtin_amdgcn_make_buffer_rsrc((void*)((p2 ? P.vB : P.vA) + (size_t)key2 * DH), 0, KVBYTES - key2 * 256u, 0x00020000);
  const int koff = ((lane >> 2) & 3) * 256 + ((lane >> 4) * 4 + (lane & 3)) * 16;
  const int voff = lane * 16;
  v4u K[NT][4], V[NT][4];
#define ISSUE_K(k_) do { _Pragma("unroll") for (int t_ = 0; t_ < 4; ++t_) \
    K[k_][t_] = __builtin_amdgcn_raw_buffer_load_b128((k_) < 4 ? rk1 : rk2, koff + t_ * 1024, (k_) * 32768, KV_AUX); FENCE_SCHED(); } while (0)
#define ISSUE_V(k_) do { _Pragma("unroll") for (int t_ = 0; t_ < 4; ++t_) \
    V[k_][t_] = __builtin_amdgcn_raw_buffer_load_b128((k_) < 4 ? rv1 : rv2, voff + t_ * 1024, (k_) * 32768, KV_AUX); FENCE_SCHED(); } while (0)
  ISSUE_K(0);
  if (tid < 4) lds_flag[tid] = 0u;


  const u32 tag0 = __builtin_amdgcn_readfirstlane(ep.x) + 1u, tag1 = __builtin_amdgcn_readfirstlane(ep.y) + 1u;
  {
    float acc[8];
#pragma unroll
    for (int j = 0; j < 8; ++j) acc[j] = 0.f;
    const float xf[4] = {bflo(xv.x), bfhi(xv.x), bflo(xv.y), bfhi(xv.y)};
#pragma unroll
    for (int k = 0; k < 4; ++k)
#pragma unroll
      for (int j = 0; j < 4; ++j) {
        acc[2 * j] = fmaf(xf[k], bflo(wv[k][j]), acc[2 * j]);
        acc[2 * j + 1] = fmaf(xf[k], bfhi(wv[k][j]), acc[2 * j + 1]);
      }
#pragma unroll
    for (int j = 0; j < 8; ++j) { acc[j] += dppf<ROR4>(acc[j]); acc[j] += dppf<ROR8>(acc[j]); }
    if (((lane >> 2) & 3) == 0) {
      float* dst = lds_red + (w * 4 + (lane >> 4)) * 32 + 8 * (lane & 3);
      *reinterpret_cast<f32x4*>(dst) = f32x4{acc[0], acc[1], acc[2], acc[3]};
      *reinterpret_cast<f32x4*>(dst + 4) = f32x4{acc[4], acc[5], acc[6], acc[7]};
    }
    __syncthreads();
    if (w == 0) {
      const int col = lane & 31, half = lane >> 5;
      float s = 0.f;
#pragma unroll
      for (int r = 0; r < 16; ++r) s += lds_red[(16 * half + r) * 32 + col];
      s = x32_sum(s);
      const u32 b = cvtpk(s, s) & 0xffffu;
      const u32 pair = b | (dppu<0xF5>(b) << 16);
      const u32 pair2 = dppu<0xAA>(pair);
      if ((lane & 3) == 0 && lane < 32) {
        const v4u gr = {pair, tag0, pair2, tag1};
        __builtin_amdgcn_raw_buffer_store_b128(gr, ws, WS_Q + ((g * 2 + pq) * 4 + hq) * 512 + (8 * dq + (lane >> 2)) * 16, 0, 16);
      }
    }
  }
  FENCE_SCHED();

  const int qh = (lane >> 2) & 3, qch = 4 * (lane >> 4) + (lane & 3);
  const bool mixed = (p1 != p2);
  int qsel = 0;
  bool need = false;
#define FLAG(n_) __builtin_amdgcn_readfirstlane(__hip_atomic_load(&lds_flag[n_], __ATOMIC_RELAXED, __HIP_MEMORY_SCOPE_WORKGROUP))
#define TAGOK(v_) ((v_).y == tag0 && (v_).w == tag1)
#define ROLE(k_) do { need = false; if ((k_) < POLL0) { } else if (w == ((k_) & 7)) { qsel = 0; need = (FLAG(0) == 0u); } else if (mixed && w == (((k_) + 4) & 7)) { qsel = 1; need = (FLAG(1) == 0u); } } while (0)
  const int qoffa = WS_Q + ((g * 2 + p1) * 4 + qh) * 512 + qch * 32, qoffb = WS_Q + ((g * 2 + p2) * 4 + qh) * 512 + qch * 32;
  v4u qa0, qa1;
#define POLLQ() do { const int qoff = qsel ? qoffb : qoffa; qa0 = __builtin_amdgcn_raw_buffer_load_b128(ws, qoff, 0, 16); qa1 = __builtin_amdgcn_raw_buffer_load_b128(ws, qoff + 16, 0, 16); FENCE_SCHED(); } while (0)
#define CHECKQ() do { if (__all(TAGOK(qa0) && TAGOK(qa1))) { *reinterpret_cast<v4u*>(lds_q + qsel * 256 + lane * 4) = v4u{qa0.x, qa0.z, qa1.x, qa1.z}; \
      asm volatile("s_waitcnt lgkmcnt(0)" ::: "memory"); __hip_atomic_store(&lds_flag[qsel], 1u, __ATOMIC_RELAXED, __HIP_MEMORY_SCOPE_WORKGROUP); } FENCE_SCHED(); } while (0)
#define PSTAGE(k_) do { ROLE(k_); if (need) POLLQ(); ISSUE_K(k_); PACE0(); if (need) CHECKQ(); } while (0)
  const int po = i >> 4, ho = (i >> 2) & 3, qo = i & 3;
  const u16* wop = (po ? P.woB : P.woA) + (size_t)((4 * g + ho) * DH + 32 * qo + 4 * w) * DM + 8 * lane;
  v4u wo[4];
#define ISSUE_W() do { _Pragma("unroll") for (int kk = 0; kk < 4; ++kk) wo[kk] = __builtin_nontemporal_load(reinterpret_cast<const v4u*>(wop + kk * DM)); FENCE_SCHED(); } while (0)
  v4u q1, q2;
#define WAITQ() do { \
    for (unsigned spins = 0; spins < (SPIN_CAP << 2); ++spins) { \
      const u32 f0 = FLAG(0), f1 = FLAG(1); \
      if (f0 != 0u && (!mixed || f1 != 0u)) break; \
      if (w == 0 && f0 == 0u) { qsel = 0; POLLQ(); CHECKQ(); } \
      else if (w == 4 && mixed && f1 == 0u) { qsel = 1; POLLQ(); CHECKQ(); } \
      else __builtin_amdgcn_s_sleep(1); } \
    asm volatile("" ::: "memory"); \
    q1 = *reinterpret_cast<const v4u*>(lds_q + lane * 4); q2 = *reinterpret_cast<const v4u*>(lds_q + (mixed ? 256 : 0) + lane * 4); } while (0)
  float s[NT][4];
#define RND(sv_) bfhi(cvtpk(bfhi(cvtpk(sv_, sv_)) * SCALE, bfhi(cvtpk(sv_, sv_)) * SCALE))
#define SCORE(Kreg_, q_) ({ const f32x4 d_ = __builtin_amdgcn_mfma_f32_16x16x32_bf16(__builtin_bit_cast(bf16x8, Kreg_), __builtin_bit_cast(bf16x8, q_), f32x4{0.f, 0.f, 0.f, 0.f}, 0, 0, 0); \
      const float sv_ = (dppf<0x00>(d_[0]) + dppf<0x55>(d_[1])) + (dppf<0xAA>(d_[2]) + dppf<0xFF>(d_[3])); RND(sv_); })
#define QK(k_) do { _Pragma("unroll") for (int t_ = 0; t_ < 4; ++t_) s[k_][t_] = SCORE(K[k_][t_], (k_) < 4 ? q1 : q2); } while (0)
  float Mc1, Mc2;
#define MAXS() do { \
    float m1 = s[0][0], m2 = s[4][0]; \
    _Pragma("unroll") for (int k = 0; k < 4; ++k) _Pragma("unroll") for (int t = 0; t < 4; ++t) m1 = fmaxf(m1, s[k][t]); \
    _Pragma("unroll") for (int k = 4; k < NT; ++k) _Pragma("unroll") for (int t = 0; t < 4; ++t) m2 = fmaxf(m2, s[k][t]); \
    m1 = xrow_max(m1); m2 = xrow_max(m2); \
    if (!mixed) { m1 = fmaxf(m1, m2); m2 = m1; } \
    Mc1 = m1 * LOG2E; Mc2 = m2 * LOG2E; \
    if ((lane & 0x33) == 0) { lds_mx[(0 * 4 + qh) * 8 + w] = Mc1; lds_mx[(1 * 4 + qh) * 8 + w] = Mc2; } } while (0)
  const u32 hm = (lane & 1) ? 0xffff0000u : 0x0000ffffu;
  const u32 mask0 = (lane & 2) ? 0u : hm, mask1 = (lane & 2) ? hm : 0u;
  float l1 = 0.f, l2 = 0.f;
  f32x4 O[2][2];
#pragma unroll
  for (int a = 0; a < 2; ++a)
#pragma unroll
    for (int b = 0; b < 2; ++b) O[a][b] = f32x4{0.f, 0.f, 0.f, 0.f};
  v2u A[NT][4];
#define PREP(k_) do { _Pragma("unroll") for (int t_ = 0; t_ < 4; ++t_) { \
      const float p_ = __builtin_amdgcn_exp2f(fmaf(s[k_][t_], LOG2E, (k_) < 4 ? -Mc1 : -Mc2)); \
      if ((k_) < 4) l1 += p_; else l2 += p_; \
      const u32 pk_ = cvtpk(p_, p_); A[k_][t_] = v2u{pk_ & mask0, pk_ & mask1}; } } while (0)
#define PV1(A_, V_, sg_) do { const s16x4 a_ = __builtin_bit_cast(s16x4, A_); \
      O[sg_][0] = __builtin_amdgcn_mfma_f32_16x16x16bf16_1k(a_, __builtin_bit_cast(s16x4, v2u{(V_).x, (V_).y}), O[sg_][0], 0, 0, 0); \
      O[sg_][1] = __builtin_amdgcn_mfma_f32_16x16x16bf16_1k(a_, __builtin_bit_cast(s16x4, v2u{(V_).z, (V_).w}), O[sg_][1], 0, 0, 0); } while (0)
#define PV(k_) do { _Pragma("unroll") for (int t_ = 0; t_ < 4; ++t_) PV1(A[k_][t_], V[k_][t_], (k_) < 4 ? 0 : 1); } while (0)
#define ENDST() do { FENCE_SCHED(); PACE0(); } while (0)

  PSTAGE(1);
  PSTAGE(2); PSTAGE(3); PSTAGE(4);
  PSTAGE(5); PSTAGE(6);
  ISSUE_K(7); WAITQ(); QK(0); QK(1); QK(2); ENDST();
  ISSUE_K(8); QK(3); QK(4); QK(5); ENDST();
  ISSUE_V(0); QK(6); QK(7); ENDST();
  ISSUE_V(1); QK(8); MAXS(); PREP(0); PV(0); PREP(1); ENDST();
  ISSUE_V(2); PV(1); PREP(2); ENDST();
  ISSUE_V(3); PV(2); PREP(3); ENDST();
  ISSUE_V(4); PV(3); PREP(4); ENDST();
  ISSUE_V(5); PV(4); PREP(5); ENDST();
  ISSUE_V(6); PV(5); PREP(6); ENDST();
  ISSUE_V(7); PV(6); PREP(7); ENDST();
  ISSUE_V(8); PV(7); PREP(8); ENDST();
  ISSUE_W(); PV(8);
  FENCE_SCHED();
  if (!mixed) { O[0][0] += O[1][0]; O[0][1] += O[1][1]; l1 += l2; }
  l1 = xrow_sum(l1); l2 = xrow_sum(l2);
  if ((lane & 0x33) == 0) { lds_l[(0 * 4 + qh) * 8 + w] = l1; lds_l[(1 * 4 + qh) * 8 + w] = l2; }
  {
    float* dst = lds_o + w * 512 + (lane >> 4) * 128 + (lane & 15) * 8;
    *reinterpret_cast<f32x4*>(dst) = O[0][0];
    *reinterpret_cast<f32x4*>(dst + 4) = O[0][1];
    if (mixed) {
      *reinterpret_cast<f32x4*>(dst + 4096) = O[1][0];
      *reinterpret_cast<f32x4*>(dst + 4096 + 4) = O[1][1];
    }
  }
  __syncthreads();
  {
    const int sg = tid >> 7;
    if (sg == 0 || (sg == 1 && mixed)) {
      const int h = (tid >> 5) & 3, gm = tid & 31;
      const int pout = sg ? p2 : p1;
      const int slot = i + pout;
      const f32x4 ma = *reinterpret_cast<const f32x4*>(lds_mx + (sg * 4 + h) * 8), mb = *reinterpret_cast<const f32x4*>(lds_mx + (sg * 4 + h) * 8 + 4);
      const float Mx = fmaxf(fmaxf(fmaxf(ma[0], ma[1]), fmaxf(ma[2], ma[3])), fmaxf(fmaxf(mb[0], mb[1]), fmaxf(mb[2], mb[3])));
      float wt[8];
#pragma unroll
      for (int ww = 0; ww < 4; ++ww) { wt[ww] = __builtin_amdgcn_exp2f(ma[ww] - Mx); wt[4 + ww] = __builtin_amdgcn_exp2f(mb[ww] - Mx); }
      f32x4 a = {0.f, 0.f, 0.f, 0.f};
#pragma unroll
      for (int ww = 0; ww < 8; ++ww) a += wt[ww] * *reinterpret_cast<const f32x4*>(lds_o + sg * 4096 + ww * 512 + h * 128 + 4 * gm);
      const int base = WS_PART + ((g * 33 + slot) * 4 + h) * PSTR;
      const v4u gr = {cvtpk(a[0], a[1]), tag0, cvtpk(a[2], a[3]), tag1};
      __builtin_amdgcn_raw_buffer_store_b128(gr, ws, base + gm * 16, 0, 16);
      if (gm == 0) {
        const f32x4 la = *reinterpret_cast<const f32x4*>(lds_l + (sg * 4 + h) * 8), lb = *reinterpret_cast<const f32x4*>(lds_l + (sg * 4 + h) * 8 + 4);
        const float L = ((wt[0] * la[0] + wt[1] * la[1]) + (wt[2] * la[2] + wt[3] * la[3])) + ((wt[4] * lb[0] + wt[5] * lb[1]) + (wt[6] * lb[2] + wt[7] * lb[3]));
        const v4u gm_ = {__float_as_uint(Mx), tag0, __float_as_uint(L), tag1};
        __builtin_amdgcn_raw_buffer_store_b128(gm_, ws, base + 32 * 16, 0, 16);
      }
    }
  }
  FENCE_SCHED();

  const int nprod = po ? 4 : 29, slot0 = po ? 29 : 0;
  {
    const bool isdata = tid < 8 * nprod, isml = !isdata && tid < 9 * nprod;
    const int j = isdata ? (tid >> 3) : (tid - 8 * nprod);
    const int off = WS_PART + ((g * 33 + slot0 + j) * 4 + ho) * PSTR + (isdata ? (8 * qo + (tid & 7)) * 16 : 32 * 16);
    if (64 * w < 9 * nprod) {
      v4u v = {0u, 0u, 0u, 0u};
      bool okv = !(isdata || isml);
      for (unsigned spins = 0; spins < SPIN_CAP; ++spins) {
        if (!okv) v = __builtin_amdgcn_raw_buffer_load_b128(ws, off, 0, 16);
        okv = okv || TAGOK(v);
        if (__all(okv)) break;
        __builtin_amdgcn_s_sleep(1);
      }
      if (isdata) {
        float* dst = lds_cv + j * 34 + 4 * (tid & 7);
        *reinterpret_cast<f32x2*>(dst) = f32x2{bflo(v.x), bfhi(v.x)};
        *reinterpret_cast<f32x2*>(dst + 2) = f32x2{bflo(v.z), bfhi(v.z)};
      } else if (isml) *reinterpret_cast<f32x2*>(lds_cml + 2 * j) = f32x2{__uint_as_float(v.x), __uint_as_float(v.z)};
    }
  }
  __syncthreads();
  {
    const int j = lane & 31, half = lane >> 5;
    const bool val = j < nprod;
    const int jj = val ? j : 0;
    const f32x2 ml = *reinterpret_cast<const f32x2*>(lds_cml + 2 * jj);
    const f32x2 vv = *reinterpret_cast<const f32x2*>(lds_cv + jj * 34 + 4 * w + 2 * half);
    float m = val ? ml[0] : -INFINITY;
    float M = m;
    M = fmaxf(M, dppf<ROR1>(M)); M = fmaxf(M, dppf<ROR2>(M)); M = fmaxf(M, dppf<ROR4>(M)); M = fmaxf(M, dppf<ROR8>(M)); M = x16_max(M);
    const float wt = val ? __builtin_amdgcn_exp2f(m - M) : 0.f;
    float L = wt * ml[1], oa = wt * vv[0], ob = wt * vv[1];
    L += dppf<ROR1>(L); oa += dppf<ROR1>(oa); ob += dppf<ROR1>(ob);
    L += dppf<ROR2>(L); oa += dppf<ROR2>(oa); ob += dppf<ROR2>(ob);
    L += dppf<ROR4>(L); oa += dppf<ROR4>(oa); ob += dppf<ROR4>(ob);
    L += dppf<ROR8>(L); oa += dppf<ROR8>(oa); ob += dppf<ROR8>(ob);
    L = x16_sum(L); oa = x16_sum(oa); ob = x16_sum(ob);
    const float inv = 1.f / L;
    oa *= inv; ob *= inv;
    { const u32 ro_ = cvtpk(oa, ob); oa = bflo(ro_); ob = bfhi(ro_); }
    float o4[4];
    o4[0] = __builtin_bit_cast(float, __builtin_amdgcn_readlane(__builtin_bit_cast(int, oa), 0));
    o4[1] = __builtin_bit_cast(float, __builtin_amdgcn_readlane(__builtin_bit_cast(int, ob), 0));
    o4[2] = __builtin_bit_cast(float, __builtin_amdgcn_readlane(__builtin_bit_cast(int, oa), 32));
    o4[3] = __builtin_bit_cast(float, __builtin_amdgcn_readlane(__builtin_bit_cast(int, ob), 32));
    float acc[8];
#pragma unroll
    for (int c = 0; c < 8; ++c) acc[c] = 0.f;
#pragma unroll
    for (int k = 0; k < 4; ++k)
#pragma unroll
      for (int c = 0; c < 4; ++c) {
        acc[2 * c] = fmaf(o4[k], bflo(wo[k][c]), acc[2 * c]);
        acc[2 * c + 1] = fmaf(o4[k], bfhi(wo[k][c]), acc[2 * c + 1]);
      }
    float* dst = lds_o + w * 512 + lane * 8;
    *reinterpret_cast<f32x4*>(dst) = f32x4{acc[0], acc[1], acc[2], acc[3]};
    *reinterpret_cast<f32x4*>(dst + 4) = f32x4{acc[4], acc[5], acc[6], acc[7]};
  }
  __syncthreads();
  if (tid < 256) {
    f32x2 a = {0.f, 0.f};
#pragma unroll
    for (int ww = 0; ww < 8; ++ww) a += *reinterpret_cast<const f32x2*>(lds_o + ww * 512 + 2 * tid);
    const v4u gr = {__float_as_uint(a[0]), tag0, __float_as_uint(a[1]), tag1};
    __builtin_amdgcn_raw_buffer_store_b128(gr, ws, WS_OUT + ((po * 128 + g * 16 + (i & 15)) * 256 + tid) * 16, 0, 16);
  }
  FENCE_SCHED();

  if (blockIdx.x < 64) {
    const int f = blockIdx.x;
    const int pr = tid >> 2, gm = tid & 3;
    const int offA = WS_OUT + ((0 * 128 + pr) * 256 + 4 * f + gm) * 16, offB = WS_OUT + ((1 * 128 + pr) * 256 + 4 * f + gm) * 16;
    v4u va = {0u, 0u, 0u, 0u}, vb = va;
    bool oka = false, okb = false;
    for (unsigned spins = 0; spins < SPIN_CAP; ++spins) {
      if (!oka) va = __builtin_amdgcn_raw_buffer_load_b128(ws, offA, 0, 16);
      if (!okb) vb = __builtin_amdgcn_raw_buffer_load_b128(ws, offB, 0, 16);
      oka = oka || TAGOK(va); okb = okb || TAGOK(vb);
      if (__all(oka && okb)) break;
      __builtin_amdgcn_s_sleep(1);
    }
    float r[4] = {__uint_as_float(va.x), __uint_as_float(va.z), __uint_as_float(vb.x), __uint_as_float(vb.z)};
#pragma unroll
    for (int c = 0; c < 4; ++c) { r[c] += dppf<ROR4>(r[c]); r[c] += dppf<ROR8>(r[c]); r[c] = xrow_sum(r[c]); }
    if (lane < 4) {
      *reinterpret_cast<f32x2*>(lds_fin + w * 16 + 0 * 8 + gm * 2) = f32x2{r[0], r[1]};
      *reinterpret_cast<f32x2*>(lds_fin + w * 16 + 1 * 8 + gm * 2) = f32x2{r[2], r[3]};
    }
    __syncthreads();
    if (tid < 16) {
      float a = 0.f;
#pragma unroll
      for (int ww = 0; ww < 8; ++ww) a += lds_fin[ww * 16 + tid];
      const int p = tid >> 3, c = tid & 7;
      P.out[p * 512 + 8 * f + c] = (u16)(cvtpk(a, a) & 0xffffu);
    }
    if (f == 0 && tid == 0) {
      const v2u ne = {tag0, tag1};
      __builtin_amdgcn_raw_buffer_store_b64(ne, ws, WS_EPOCH, 0, 16);
    }
  }
}

extern "C" void kernel_launch(void* const* d_in, const int* in_sizes, int n_in, void* d_out, int out_size, void* d_ws, size_t ws_size,
                              hipStream_t stream) {
  Params P;
  P.xA = (const u16*)d_in[0]; P.kA = (const u16*)d_in[1]; P.vA = (const u16*)d_in[2]; P.wqA = (const u16*)d_in[3]; P.woA = (const u16*)d_in[4];
  P.xB = (const u16*)d_in[5]; P.kB = (const u16*)d_in[6]; P.vB = (const u16*)d_in[7]; P.wqB = (const u16*)d_in[8]; P.woB = (const u16*)d_in[9];
  P.out = (u16*)d_out; P.ws = (unsigned char*)d_ws;
  hipLaunchKernelGGL(fused_decode_kernel, dim3(NWG), dim3(NTHR), 0, stream, P);
}
```
